```python
import torch

def _softmax(x, axis=-1):
    return torch.softmax(x, dim=axis)
def _gelu(x):
    return torch.nn.functional.gelu(x, approximate="tanh")
def _silu(x):
    return torch.nn.functional.silu(x)
def _sigmoid(x):
    return torch.sigmoid(x)
def _layernorm(x, axis=-1, eps=1e-5):
    m = x.mean(dim=axis, keepdim=True)
    v = x.var(dim=axis, keepdim=True, unbiased=False)
    return (x - m) / torch.sqrt(v + eps)
def _scatter_add(tgt, idx, src, axis=0):
    return tgt.clone().index_add_(axis, idx, src)
def _segment_sum(src, seg, n):
    out = torch.zeros((n,) + src.shape[1:], dtype=src.dtype)
    return out.index_add_(0, seg, src)
def _pad(x, pw):
    flat = tuple(v for pair in reversed(pw) for v in pair)
    return torch.nn.functional.pad(x, flat)

kernel_name = 'interleave_layers__mha_block_cached__glu_gated_geglu__decode'

def setup_inputs():

    _w = (lambda a: (lambda t: t.to(torch.bfloat16) if t.is_floating_point() else t)(torch.as_tensor(a)))
    def _A_setup():
        B, Sq, D = 4, 1, 640
        H, Sk, Dh = 32, 8192, 192
        return {
            "x": _w((lambda *s: torch.randn(*s, dtype=torch.bfloat16) * 0.05)(B, Sq, D)),
            "k_cache": _w((lambda *s: torch.randn(*s, dtype=torch.bfloat16) * 0.4)(B, H, Sk, Dh)),
            "v_cache": _w((lambda *s: torch.randn(*s, dtype=torch.bfloat16) * 1.6)(B, H, Sk, Dh)),
            "wq": _w((lambda *s: torch.randn(*s, dtype=torch.bfloat16) * 0.4)(D, H * Dh) * (D ** -0.5)),
            "wo": _w((lambda *s: torch.randn(*s, dtype=torch.bfloat16) * 1.6)(H * Dh, D) * ((H * Dh) ** -0.5)),
        }
    _A_in = _A_setup()

    def _B_setup():
        B, S, Dm, Df = 4, 1, 640, 1536
        return {
            "x": _w((lambda *s: torch.randn(*s, dtype=torch.bfloat16) * 0.05)(B, S, Dm)),
            "w_gate": _w((lambda *s: torch.randn(*s, dtype=torch.bfloat16) * 0.4)(Dm, Df) * (Dm ** -0.5)),
            "w_up": _w((lambda *s: torch.randn(*s, dtype=torch.bfloat16) * 0.4)(Dm, Df) * (Dm ** -0.5)),
            "w_down": _w((lambda *s: torch.randn(*s, dtype=torch.bfloat16) * 0.4)(Df, Dm) * (Df ** -0.5)),
        }
    _B_in = _B_setup()

    return {
        "A_x": _A_in["x"],
        "A_k_cache": _A_in["k_cache"],
        "A_v_cache": _A_in["v_cache"],
        "A_wq": _A_in["wq"],
        "A_wo": _A_in["wo"],
        "B_x": _B_in["x"],
        "B_w_gate": _B_in["w_gate"],
        "B_w_up": _B_in["w_up"],
        "B_w_down": _B_in["w_down"],
    }

def reference(A_x, A_k_cache, A_v_cache, A_wq, A_wo, B_x, B_w_gate, B_w_up, B_w_down):
    def _A_ref(x, k_cache, v_cache, wq, wo):
        B, Sq, D = x.shape
        H, Dh = 32, 192
        q = (lambda x, *d: x.permute(*d))((x @ wq).reshape(B, Sq, H, Dh), 0, 2, 1, 3)
        scores = torch.einsum("bhqd,bhkd->bhqk", q, k_cache) * (Dh ** -0.5)
        p = _softmax(scores, axis=-1)
        o = torch.einsum("bhqk,bhkd->bhqd", p, v_cache)
        return (lambda x, *d: x.permute(*d))(o, 0, 2, 1, 3).reshape(B, Sq, H * Dh) @ wo

    def _B_ref(x, w_gate, w_up, w_down):
        g = torch.einsum("bsd,df->bsf", x, w_gate)
        u = torch.einsum("bsd,df->bsf", x, w_up)
        h = _gelu(g) * u
        return torch.einsum("bsf,fd->bsd", h, w_down)

    x = A_x
    x = x + _A_ref(_layernorm(x, axis=-1), A_k_cache, A_v_cache, A_wq, A_wo)
    x = x + _B_ref(_layernorm(x, axis=-1), B_w_gate, B_w_up, B_w_down)
    x = x + _A_ref(_layernorm(x, axis=-1), A_k_cache, A_v_cache, A_wq, A_wo)
    x = x + _B_ref(_layernorm(x, axis=-1), B_w_gate, B_w_up, B_w_down)
    return x
```

```cpp
#include <hip/hip_runtime.h>
#include <hip/hip_bf16.h>
#include <stdint.h>

typedef unsigned v4u __attribute__((ext_vector_type(4)));
typedef float f32x4 __attribute__((ext_vector_type(4)));
typedef __attribute__((ext_vector_type(2))) __bf16 bf16x2;

constexpr int B = 4, D = 640, H = 32, SK = 8192, DH = 192, DF = 1536, HD = H * DH;
#define NSPLIT 4
#define KV_AUX1 2
#define KV_AUX2 2

__device__ __forceinline__ float bf2f(unsigned short u) { return __builtin_bit_cast(float, (unsigned)u << 16); }
__device__ __forceinline__ float bflo(unsigned u) { return __builtin_bit_cast(float, u << 16); }
__device__ __forceinline__ float bfhi(unsigned u) { return __builtin_bit_cast(float, u & 0xffff0000u); }
__device__ __forceinline__ unsigned short f2bf(float f) { __bf16 b = (__bf16)f; return __builtin_bit_cast(unsigned short, b); }
__device__ __forceinline__ float rbf(float f) { return bf2f(f2bf(f)); }
__device__ __forceinline__ float dot2(unsigned k, unsigned q, float acc) {
  return __builtin_amdgcn_fdot2_f32_bf16(__builtin_bit_cast(bf16x2, k), __builtin_bit_cast(bf16x2, q), acc, false);
}
template <int CTRL> __device__ __forceinline__ float dpp(float x) {
  return __builtin_bit_cast(float, __builtin_amdgcn_mov_dpp(__builtin_bit_cast(int, x), CTRL, 0xf, 0xf, true));
}
constexpr int XOR1 = 0xB1, XOR2 = 0x4E, XOR7 = 0x141, XOR8 = 0x128;
__device__ __forceinline__ float wave_sum(float v) { for (int o = 32; o; o >>= 1) v += __shfl_xor(v, o); return v; }
__device__ __forceinline__ float wave_max(float v) { for (int o = 32; o; o >>= 1) v = fmaxf(v, __shfl_xor(v, o)); return v; }

__device__ __forceinline__ void ln_rows(const float* x, float* xn) {
  const int w = threadIdx.x >> 6, lane = threadIdx.x & 63;
  if (w < 4) {
    float v[10]; float s = 0.f;
#pragma unroll
    for (int i = 0; i < 10; ++i) { v[i] = x[w * D + lane + 64 * i]; s += v[i]; }
    s = wave_sum(s); const float m = s * (1.f / D);
    float q = 0.f;
#pragma unroll
    for (int i = 0; i < 10; ++i) { const float d = v[i] - m; q += d * d; }
    q = wave_sum(q); const float var = q * (1.f / D);
    const float r = 1.f / sqrtf(var + 1e-5f);
#pragma unroll
    for (int i = 0; i < 10; ++i) xn[w * D + lane + 64 * i] = rbf((v[i] - m) * r);
  }
  __syncthreads();
}

__global__ void k_init(const unsigned short* x, float* xf) { int i = blockIdx.x * 256 + threadIdx.x; if (i < B * D) xf[i] = bf2f(x[i]); }

__global__ void __launch_bounds__(256) k_qproj(const float* x, const unsigned short* wq, unsigned short* q) {
  __shared__ float xn[B * D];
  __shared__ float red[64][4][33];
  ln_rows(x, xn);
  const int cg = threadIdx.x & 3, ks = threadIdx.x >> 2;
  const int c0 = blockIdx.x * 32 + cg * 8;
  float acc[4][8] = {};
#pragma unroll
  for (int i = 0; i < 10; ++i) {
    const int k = ks + 64 * i;
    const v4u w = *(const v4u*)(wq + (size_t)k * HD + c0);
    float wf[8] = {bflo(w.x), bfhi(w.x), bflo(w.y), bfhi(w.y), bflo(w.z), bfhi(w.z), bflo(w.w), bfhi(w.w)};
#pragma unroll
    for (int b = 0; b < 4; ++b) { const float xv = xn[b * D + k];
#pragma unroll
      for (int j = 0; j < 8; ++j) acc[b][j] = fmaf(xv, wf[j], acc[b][j]); }
  }
#pragma unroll
  for (int b = 0; b < 4; ++b)
#pragma unroll
    for (int j = 0; j < 8; ++j) red[ks][b][cg * 8 + j] = acc[b][j];
  __syncthreads();
  if (threadIdx.x < 128) { const int b = threadIdx.x >> 5, c = threadIdx.x & 31; float s = 0.f;
    for (int k = 0; k < 64; ++k) s += red[k][b][c];
    q[b * HD + blockIdx.x * 32 + c] = f2bf(s); }
}

template <int AUX>
__global__ void __launch_bounds__(512) k_attn(const unsigned short* q, const char* K, const char* V, float* part) {
  constexpr int NW = 8, KEYS = SK / NSPLIT, KPW = KEYS / NW;
  constexpr int T = 4;
  __shared__ float sm[NW][DH + 2];
  const int hd = blockIdx.y, sp = blockIdx.x;
  const int wave = __builtin_amdgcn_readfirstlane(threadIdx.x >> 6), lane = threadIdx.x & 63, r = lane >> 3, i = lane & 7;
  const size_t base = ((size_t)hd * SK + sp * KEYS + wave * KPW) * (DH * 2);
  __amdgpu_buffer_rsrc_t rk = __builtin_amdgcn_make_buffer_rsrc((void*)(K + base), 0, KPW * DH * 2, 0x00020000);
  __amdgpu_buffer_rsrc_t rv = __builtin_amdgcn_make_buffer_rsrc((void*)(V + base), 0, KPW * DH * 2, 0x00020000);
  v4u qv[3];
#pragma unroll
  for (int c = 0; c < 3; ++c) qv[c] = *(const v4u*)(q + hd * DH + (i + 8 * c) * 8);
  const int loff = r * 384 + i * 16;
  constexpr float C2 = 0.07216878364870322f * 1.4426950408889634f;
  float m = -INFINITY, l = 0.f, acc[24];
#pragma unroll
  for (int k = 0; k < 24; ++k) acc[k] = 0.f;
  v4u kn[T][3], vn[T][3];
#define ISSUE(t0) _Pragma("unroll") for (int t = 0; t < T; ++t) _Pragma("unroll") for (int c = 0; c < 3; ++c) { \
    kn[t][c] = __builtin_amdgcn_raw_buffer_load_b128(rk, ((t0) + t) * 3072 + loff + c * 128, 0, AUX); } \
    _Pragma("unroll") for (int t = 0; t < T; ++t) _Pragma("unroll") for (int c = 0; c < 3; ++c) { \
    vn[t][c] = __builtin_amdgcn_raw_buffer_load_b128(rv, ((t0) + t) * 3072 + loff + c * 128, 0, AUX); }
  ISSUE(0);
  for (int g = 0; g < KPW / 8; g += T) {
    v4u kc[T][3], vc[T][3];
#pragma unroll
    for (int t = 0; t < T; ++t)
#pragma unroll
      for (int c = 0; c < 3; ++c) { kc[t][c] = kn[t][c]; vc[t][c] = vn[t][c]; }
    if (g + T < KPW / 8) { ISSUE(g + T); }
    float s[T];
#pragma unroll
    for (int t = 0; t < T; ++t) { float d = 0.f;
#pragma unroll
      for (int c = 0; c < 3; ++c) { d = dot2(kc[t][c].x, qv[c].x, d); d = dot2(kc[t][c].y, qv[c].y, d); d = dot2(kc[t][c].z, qv[c].z, d); d = dot2(kc[t][c].w, qv[c].w, d); }
      d += dpp<XOR1>(d); d += dpp<XOR2>(d); d += dpp<XOR7>(d);
      s[t] = d * C2; }
    float mx = m;
#pragma unroll
    for (int t = 0; t < T; ++t) mx = fmaxf(mx, s[t]);
    const float alpha = __builtin_amdgcn_exp2f(m - mx);
    m = mx; l *= alpha;
#pragma unroll
    for (int k = 0; k < 24; ++k) acc[k] *= alpha;
#pragma unroll
    for (int t = 0; t < T; ++t) { const float p = __builtin_amdgcn_exp2f(s[t] - m); l += p;
#pragma unroll
      for (int c = 0; c < 3; ++c) {
        acc[c*8+0] = fmaf(p, bflo(vc[t][c].x), acc[c*8+0]); acc[c*8+1] = fmaf(p, bfhi(vc[t][c].x), acc[c*8+1]);
        acc[c*8+2] = fmaf(p, bflo(vc[t][c].y), acc[c*8+2]); acc[c*8+3] = fmaf(p, bfhi(vc[t][c].y), acc[c*8+3]);
        acc[c*8+4] = fmaf(p, bflo(vc[t][c].z), acc[c*8+4]); acc[c*8+5] = fmaf(p, bfhi(vc[t][c].z), acc[c*8+5]);
        acc[c*8+6] = fmaf(p, bflo(vc[t][c].w), acc[c*8+6]); acc[c*8+7] = fmaf(p, bfhi(vc[t][c].w), acc[c*8+7]); } }
  }
  const float M = wave_max(m);
  const float sc = __builtin_amdgcn_exp2f(m - M);
  l *= sc;
#pragma unroll
  for (int k = 0; k < 24; ++k) { float a = acc[k] * sc; a += __shfl_xor(a, 8); a += __shfl_xor(a, 16); a += __shfl_xor(a, 32); acc[k] = a; }
  l += __shfl_xor(l, 8); l += __shfl_xor(l, 16); l += __shfl_xor(l, 32);
  if (r == 0) {
#pragma unroll
    for (int c = 0; c < 3; ++c)
#pragma unroll
      for (int k = 0; k < 8; ++k) sm[wave][(i + 8 * c) * 8 + k] = acc[c * 8 + k];
    if (i == 0) { sm[wave][DH] = M; sm[wave][DH + 1] = l; }
  }
  __syncthreads();
  if (threadIdx.x < DH) {
    float MM = -INFINITY;
    for (int w = 0; w < NW; ++w) MM = fmaxf(MM, sm[w][DH]);
    float L = 0.f, o = 0.f;
    for (int w = 0; w < NW; ++w) { const float f = __builtin_amdgcn_exp2f(sm[w][DH] - MM); L += f * sm[w][DH + 1]; o += f * sm[w][threadIdx.x]; }
    float* p = part + ((size_t)hd * NSPLIT + sp) * 256;
    p[threadIdx.x] = o;
    if (threadIdx.x == 0) { p[DH] = MM; p[DH + 1] = L; }
  }
}

__global__ void __launch_bounds__(256) k_wo(const float* part, const unsigned short* wo, float* ypart) {
  __shared__ float o[4][DH];
  __shared__ float red[16][4][129];
  const int h = blockIdx.x, cb = blockIdx.y;
  for (int t = threadIdx.x; t < 4 * DH; t += 256) { const int b = t / DH, d = t % DH; const float* p = part + (size_t)(b * H + h) * NSPLIT * 256;
    float MM = -INFINITY; for (int s = 0; s < NSPLIT; ++s) MM = fmaxf(MM, p[s * 256 + DH]);
    float L = 0.f, a = 0.f; for (int s = 0; s < NSPLIT; ++s) { const float f = __builtin_amdgcn_exp2f(p[s * 256 + DH] - MM); L += f * p[s * 256 + DH + 1]; a += f * p[s * 256 + d]; }
    o[b][d] = rbf(a / L); }
  __syncthreads();
  const int cg = threadIdx.x & 15, ks = threadIdx.x >> 4;
  float acc[4][8] = {};
#pragma unroll
  for (int i = 0; i < 12; ++i) { const int k = ks + 16 * i;
    const v4u w = *(const v4u*)(wo + (size_t)(h * DH + k) * D + cb * 128 + cg * 8);
    float wf[8] = {bflo(w.x), bfhi(w.x), bflo(w.y), bfhi(w.y), bflo(w.z), bfhi(w.z), bflo(w.w), bfhi(w.w)};
#pragma unroll
    for (int b = 0; b < 4; ++b) { const float xv = o[b][k];
#pragma unroll
      for (int j = 0; j < 8; ++j) acc[b][j] = fmaf(xv, wf[j], acc[b][j]); } }
#pragma unroll
  for (int b = 0; b < 4; ++b)
#pragma unroll
    for (int j = 0; j < 8; ++j) red[ks][b][cg * 8 + j] = acc[b][j];
  __syncthreads();
  for (int t = threadIdx.x; t < 512; t += 256) { const int b = t >> 7, c = t & 127; float s = 0.f; for (int k = 0; k < 16; ++k) s += red[k][b][c];
    ypart[((size_t)h * 4 + b) * D + cb * 128 + c] = s; }
}
__global__ void k_resid(float* x, const float* ypart, int np, unsigned short* out) {
  const int i = blockIdx.x * 256 + threadIdx.x; if (i >= B * D) return;
  float s = 0.f; for (int p = 0; p < np; ++p) s += ypart[(size_t)p * B * D + i];
  const float r = rbf(x[i] + rbf(s)); x[i] = r; if (out) out[i] = f2bf(r);
}
__global__ void __launch_bounds__(256) k_ffn1(const float* x, const unsigned short* wg, const unsigned short* wu, float* hbuf) {
  __shared__ float xn[B * D];
  __shared__ float red[64][4][33];
  ln_rows(x, xn);
  const int cg = threadIdx.x & 1, mat = (threadIdx.x >> 1) & 1, ks = threadIdx.x >> 2;
  const unsigned short* W = mat ? wu : wg;
  const int c0 = blockIdx.x * 16 + cg * 8;
  float acc[4][8] = {};
#pragma unroll
  for (int i = 0; i < 10; ++i) { const int k = ks + 64 * i;
    const v4u w = *(const v4u*)(W + (size_t)k * DF + c0);
    float wf[8] = {bflo(w.x), bfhi(w.x), bflo(w.y), bfhi(w.y), bflo(w.z), bfhi(w.z), bflo(w.w), bfhi(w.w)};
#pragma unroll
    for (int b = 0; b < 4; ++b) { const float xv = xn[b * D + k];
#pragma unroll
      for (int j = 0; j < 8; ++j) acc[b][j] = fmaf(xv, wf[j], acc[b][j]); } }
#pragma unroll
  for (int b = 0; b < 4; ++b)
#pragma unroll
    for (int j = 0; j < 8; ++j) red[ks][b][mat * 16 + cg * 8 + j] = acc[b][j];
  __syncthreads();
  if (threadIdx.x < 64) { const int b = threadIdx.x >> 4, c = threadIdx.x & 15; float g = 0.f, u = 0.f;
    for (int k = 0; k < 64; ++k) { g += red[k][b][c]; u += red[k][b][16 + c]; }
    g = rbf(g); u = rbf(u);
    const float t = tanhf(0.7978845608028654f * (g + 0.044715f * g * g * g));
    const float ge = rbf(0.5f * g * (1.f + t));
    hbuf[b * DF + blockIdx.x * 16 + c] = rbf(ge * u); }
}
__global__ void __launch_bounds__(256) k_ffn2(const float* hbuf, const unsigned short* wd, float* ypart) {
  __shared__ float o[4][DH];
  __shared__ float red[16][4][129];
  const int h = blockIdx.x, cb = blockIdx.y;
  for (int t = threadIdx.x; t < 4 * DH; t += 256) { const int b = t / DH, d = t % DH; o[b][d] = hbuf[b * DF + h * DH + d]; }
  __syncthreads();
  const int cg = threadIdx.x & 15, ks = threadIdx.x >> 4;
  float acc[4][8] = {};
#pragma unroll
  for (int i = 0; i < 12; ++i) { const int k = ks + 16 * i;
    const v4u w = *(const v4u*)(wd + (size_t)(h * DH + k) * D + cb * 128 + cg * 8);
    float wf[8] = {bflo(w.x), bfhi(w.x), bflo(w.y), bfhi(w.y), bflo(w.z), bfhi(w.z), bflo(w.w), bfhi(w.w)};
#pragma unroll
    for (int b = 0; b < 4; ++b) { const float xv = o[b][k];
#pragma unroll
      for (int j = 0; j < 8; ++j) acc[b][j] = fmaf(xv, wf[j], acc[b][j]); } }
#pragma unroll
  for (int b = 0; b < 4; ++b)
#pragma unroll
    for (int j = 0; j < 8; ++j) red[ks][b][cg * 8 + j] = acc[b][j];
  __syncthreads();
  for (int t = threadIdx.x; t < 512; t += 256) { const int b = t >> 7, c = t & 127; float s = 0.f; for (int k = 0; k < 16; ++k) s += red[k][b][c];
    ypart[((size_t)h * 4 + b) * D + cb * 128 + c] = s; }
}

extern "C" void kernel_launch(void* const* d_in, const int* in_sizes, int n_in, void* d_out, int out_size, void* d_ws, size_t ws_size, hipStream_t stream) {
  const unsigned short* x = (const unsigned short*)d_in[0];
  const char* K = (const char*)d_in[1]; const char* V = (const char*)d_in[2];
  const unsigned short* wq = (const unsigned short*)d_in[3]; const unsigned short* wo = (const unsigned short*)d_in[4];
  const unsigned short* wg = (const unsigned short*)d_in[6]; const unsigned short* wu = (const unsigned short*)d_in[7]; const unsigned short* wd = (const unsigned short*)d_in[8];
  char* ws = (char*)d_ws;
  float* xf = (float*)ws; unsigned short* q = (unsigned short*)(ws + 16384); float* hbuf = (float*)(ws + 131072);
  float* ypart = (float*)(ws + 262144); float* part = (float*)(ws + 1048576);
  k_init<<<10, 256, 0, stream>>>(x, xf);
  for (int layer = 0; layer < 2; ++layer) {
    k_qproj<<<HD / 32, 256, 0, stream>>>(xf, wq, q);
    if (layer == 0) k_attn<KV_AUX1><<<dim3(NSPLIT, B * H), 512, 0, stream>>>(q, K, V, part);
    else k_attn<KV_AUX2><<<dim3(NSPLIT, B * H), 512, 0, stream>>>(q, K, V, part);
    k_wo<<<dim3(32, 5), 256, 0, stream>>>(part, wo, ypart);
    k_resid<<<10, 256, 0, stream>>>(xf, ypart, 32, nullptr);
    k_ffn1<<<96, 256, 0, stream>>>(xf, wg, wu, hbuf);
    k_ffn2<<<dim3(8, 5), 256, 0, stream>>>(hbuf, wd, ypart);
    k_resid<<<10, 256, 0, stream>>>(xf, ypart, 8, layer == 1 ? (unsigned short*)d_out : nullptr);
  }
}
```

```cpp
#include <hip/hip_runtime.h>
#include <hip/hip_bf16.h>
#include <stdint.h>

typedef unsigned v4u __attribute__((ext_vector_type(4)));
typedef unsigned v2u __attribute__((ext_vector_type(2)));
typedef float f32x4 __attribute__((ext_vector_type(4)));
typedef __attribute__((ext_vector_type(2))) __bf16 bf16x2;
typedef __amdgpu_buffer_rsrc_t rsrc_t;

namespace {
constexpr int DM = 640, NH = 32, SK = 8192, DH = 192, DF = 1536, HD = NH * DH;
constexpr unsigned HEAD_BYTES = SK * DH * 2;
constexpr unsigned KV_BYTES = 4u * NH * HEAD_BYTES;
constexpr int NR = 21, NN = 17;
constexpr unsigned W_ERR = 0, W_HS = 1024, W_Q = 8192;
constexpr unsigned Q_PLANE = 32768, XWAVE = 4608, PART_SLOT = 1024, PART_HEAD = 8192, PART_PLANE = 262144;
constexpr unsigned W_Y = W_Q + 8 * Q_PLANE, W_Z = W_Y + 32 * 2560;
constexpr unsigned W_X = W_Z + 32 * 2560;
constexpr unsigned W_PART = W_X + 224 * XWAVE;
constexpr unsigned W_ZERO_END = W_PART + 4 * PART_PLANE;
constexpr unsigned W_PROBE = W_ZERO_END, W_DBG = W_PROBE + 8192, W_END = W_DBG + 1024 * 256;
static_assert(W_ZERO_END % 16 == 0, "ws");
constexpr int NPG = 200;
constexpr unsigned SPIN_CAP = 1u << 17;
constexpr int L_KSLOT = 0, L_KW = 33792, L_MISC = 4 * L_KW, WAVE_MISC = 3968, L_DESC = L_MISC + 4 * WAVE_MISC, L_CU = L_DESC + 2048, L_TOTAL = 163840;
static_assert(L_CU + 4608 <= L_TOTAL, "lds");
constexpr int C_RED = 0, C_XN = C_RED + 10240, C_XM = C_XN + 1280, C_ST = C_XM + 1280, C_RED2 = C_RED + 8704, C_XS = C_ST + 64, C_WQ = 16384, C_WGU = C_WQ + 24576, C_END = C_WGU + 122880;
static_assert(C_END <= L_TOTAL && C_XS + 2560 <= C_WQ, "lds");

__device__ __forceinline__ float bflo(unsigned u) { return __builtin_bit_cast(float, u << 16); }
__device__ __forceinline__ float bfhi(unsigned u) { return __builtin_bit_cast(float, u & 0xffff0000u); }
__device__ __forceinline__ unsigned f2bf(float f) { __bf16 b = (__bf16)f; return (unsigned)__builtin_bit_cast(unsigned short, b); }
__device__ __forceinline__ unsigned pk2(float lo, float hi) { return f2bf(lo) | (f2bf(hi) << 16); }
__device__ __forceinline__ float rbf(float f) { return bflo(f2bf(f)); }
__device__ __forceinline__ float dot2(unsigned a, unsigned b, float acc) {
  return __builtin_amdgcn_fdot2_f32_bf16(__builtin_bit_cast(bf16x2, a), __builtin_bit_cast(bf16x2, b), acc, false);
}
template <int CTRL> __device__ __forceinline__ float dpp(float x) {
  return __builtin_bit_cast(float, __builtin_amdgcn_mov_dpp(__builtin_bit_cast(int, x), CTRL, 0xf, 0xf, true));
}
__device__ __forceinline__ float x16sum(float x) {
  auto s = __builtin_amdgcn_permlane16_swap(__float_as_uint(x), __float_as_uint(x), false, false);
  return __uint_as_float(s[0]) + __uint_as_float(s[1]);
}
__device__ __forceinline__ float x32sum(float x) {
  auto s = __builtin_amdgcn_permlane32_swap(__float_as_uint(x), __float_as_uint(x), false, false);
  return __uint_as_float(s[0]) + __uint_as_float(s[1]);
}
__device__ __forceinline__ float rs32(float a, float b) {
  auto s = __builtin_amdgcn_permlane32_swap(__float_as_uint(a), __float_as_uint(b), false, false);
  return __uint_as_float(s[0]) + __uint_as_float(s[1]);
}
__device__ __forceinline__ float rs16(float a, float b) {
  auto s = __builtin_amdgcn_permlane16_swap(__float_as_uint(a), __float_as_uint(b), false, false);
  return __uint_as_float(s[0]) + __uint_as_float(s[1]);
}
__device__ __forceinline__ float wave_sum(float v) {
  v += dpp<0xB1>(v); v += dpp<0x4E>(v); v += dpp<0x141>(v); v += dpp<0x140>(v);
  return x32sum(x16sum(v));
}
__device__ __forceinline__ v4u ld16(rsrc_t r, unsigned off) { return __builtin_amdgcn_raw_buffer_load_b128(r, (int)off, 0, 16); }
__device__ __forceinline__ v2u ld8(rsrc_t r, unsigned off) { return __builtin_amdgcn_raw_buffer_load_b64(r, (int)off, 0, 16); }
__device__ __forceinline__ unsigned ld4(rsrc_t r, unsigned off) { return __builtin_amdgcn_raw_buffer_load_b32(r, (int)off, 0, 16); }
__device__ __forceinline__ void st16(rsrc_t r, unsigned off, v4u v) { __builtin_amdgcn_raw_buffer_store_b128(v, r, (int)off, 0, 16); }
__device__ __forceinline__ void st8(rsrc_t r, unsigned off, v2u v) { __builtin_amdgcn_raw_buffer_store_b64(v, r, (int)off, 0, 16); }
__device__ __forceinline__ void st4(rsrc_t r, unsigned off, unsigned v) { __builtin_amdgcn_raw_buffer_store_b32(v, r, (int)off, 0, 16); }
#define DRAIN() asm volatile("s_waitcnt vmcnt(0)" ::: "memory")
#define ORDER() __builtin_amdgcn_fence(__ATOMIC_ACQUIRE, "wavefront")
__device__ __forceinline__ unsigned xcc_id() { return (unsigned)__builtin_amdgcn_s_getreg((3 << 11) | 20) & 0xFu; }
__device__ __forceinline__ bool spin_fail(unsigned& spins, rsrc_t rw, unsigned code) {
  if (++spins > SPIN_CAP) { st4(rw, W_ERR, code); return true; }
  if ((spins & 63u) == 0u && __builtin_amdgcn_readfirstlane(ld4(rw, W_ERR)) != 0u) return true;
  __builtin_amdgcn_s_sleep(1);
  return false;
}
__device__ __forceinline__ int class_row(int c, int i) {
  int acc = 0, res = 0;
#pragma unroll
  for (int k = 0; k < 3; ++k) { const int bk = c + 8 * k, f = (bk * 32 + 1) / 3, n = ((bk + 1) * 32 + 1) / 3 - f;
    if (i >= acc && i < acc + n) res = f + (i - acc);
    acc += n; }
  return res;
}

struct Args { const unsigned short* x; const char* k; const char* v; const unsigned* wq; const unsigned* wo; const unsigned* wg; const unsigned* wu; const unsigned* wd; unsigned short* out; char* ws; };

#define CSTAMP(id) do {} while (0)

#define A_CLOB "a0","a1","a2","a3","a4","a5","a6","a7","a8","a9","a10","a11","a12","a13","a14","a15","a16","a17","a18","a19","a20","a21","a22","a23","a24","a25","a26","a27","a28","a29","a30","a31","a32","a33","a34","a35","a36","a37","a38","a39","a40","a41","a42","a43","a44","a45","a46","a47"
#define GV_CLOB A_CLOB,"s36","s37","s38","s39","s40","s41","s42","s43","s44","s45","s46","s47","vcc","scc","memory"
__device__ __forceinline__ unsigned tagw(float v, unsigned tag) { return (__float_as_uint(v) & ~15u) | tag; }
__device__ __forceinline__ v4u mk_rsrc(const void* p, unsigned bytes) {
  const unsigned long long u = (unsigned long long)p;
  return v4u{(unsigned)u, (unsigned)(u >> 32), bytes, 0x00020000u};
}
__device__ __forceinline__ float x16max(float x) {
  auto s = __builtin_amdgcn_permlane16_swap(__float_as_uint(x), __float_as_uint(x), false, false);
  return fmaxf(__uint_as_float(s[0]), __uint_as_float(s[1]));
}
__device__ __forceinline__ float x32max(float x) {
  auto s = __builtin_amdgcn_permlane32_swap(__float_as_uint(x), __float_as_uint(x), false, false);
  return fmaxf(__uint_as_float(s[0]), __uint_as_float(s[1]));
}

__device__ __forceinline__ void chain_main(const Args& a, char* lds, int j) {
  int t = threadIdx.x, lane = t & 63;
  const int w = __builtin_amdgcn_readfirstlane(t >> 6);
  const rsrc_t rw = __builtin_amdgcn_make_buffer_rsrc((void*)a.ws, 0, (int)W_END, 0x00020000);
  float* red = (float*)(lds + C_RED);
  unsigned* xn = (unsigned*)(lds + C_XN);
  unsigned* xs = (unsigned*)(lds + C_XS);
  unsigned* xm = (unsigned*)(lds + C_XM);
  const unsigned ldsb = (unsigned)(unsigned long long)(__attribute__((address_space(3))) char*)lds;
  int hw = t >> 5, l32 = t & 31;
#define REFRESH() do { asm volatile("" : "+v"(t) :: A_CLOB); lane = t & 63; hw = t >> 5; l32 = t & 31; } while (0)
#define wq_off ((80 * hw * (HD / 2) + 96 * j + l32) * 4)
#define wo_off (((192 * j + 8 * w) * (DM / 2) + lane) * 4)
#define wd_off (((48 * j + 12 * w) * (DM / 2) + lane) * 4)
  const v4u rw4 = mk_rsrc(a.ws, W_END);
  const v4u rq = mk_rsrc(a.wq, DM * HD * 2), ro = mk_rsrc(a.wo, DM * HD * 2), rd = mk_rsrc(a.wd, DM * DF * 2);
#define sel0 ((t & 3) == 0 ? 0x0c0c0100u : ((t & 3) == 1 ? 0x01000c0cu : 0x0c0c0c0cu))
#define sel1 ((t & 3) == 2 ? 0x0c0c0302u : ((t & 3) == 3 ? 0x03020c0cu : 0x0c0c0c0cu))
#define LOAD_WQ_T() asm volatile(     "s_mov_b32 s36, 700416\n" "buffer_load_dword v130, %0, %1, s36 offen offset:0\n" "buffer_load_dword v132, %0, %1, s36 offen offset:128\n" "buffer_load_dword v134, %0, %1, s36 offen offset:256\n" "s_mov_b32 s36, 724992\n" "buffer_load_dword v131, %0, %1, s36 offen offset:0\n" "buffer_load_dword v133, %0, %1, s36 offen offset:128\n" "buffer_load_dword v135, %0, %1, s36 offen offset:256\n" "s_mov_b32 s36, 737280\n" "buffer_load_dword v136, %0, %1, s36 offen offset:0\n" "buffer_load_dword v138, %0, %1, s36 offen offset:128\n" "buffer_load_dword v140, %0, %1, s36 offen offset:256\n" "s_mov_b32 s36, 761856\n" "buffer_load_dword v137, %0, %1, s36 offen offset:0\n" "buffer_load_dword v139, %0, %1, s36 offen offset:128\n" "buffer_load_dword v141, %0, %1, s36 offen offset:256\n" "s_mov_b32 s36, 749568\n" "buffer_load_dword v142, %0, %1, s36 offen offset:0\n" "buffer_load_dword v144, %0, %1, s36 offen offset:128\n" "buffer_load_dword v146, %0, %1, s36 offen offset:256\n" "s_mov_b32 s36, 774144\n" "buffer_load_dword v143, %0, %1, s36 offen offset:0\n" "buffer_load_dword v145, %0, %1, s36 offen offset:128\n" "buffer_load_dword v147, %0, %1, s36 offen offset:256\n" "s_mov_b32 s36, 786432\n" "buffer_load_dword v148, %0, %1, s36 offen offset:0\n" "buffer_load_dword v150, %0, %1, s36 offen offset:128\n" "buffer_load_dword v152, %0, %1, s36 offen offset:256\n" "s_mov_b32 s36, 811008\n" "buffer_load_dword v149, %0, %1, s36 offen offset:0\n" "buffer_load_dword v151, %0, %1, s36 offen offset:128\n" "buffer_load_dword v153, %0, %1, s36 offen offset:256\n" "s_mov_b32 s36, 798720\n" "buffer_load_dword v154, %0, %1, s36 offen offset:0\n" "buffer_load_dword v156, %0, %1, s36 offen offset:128\n" "buffer_load_dword v158, %0, %1, s36 offen offset:256\n" "s_mov_b32 s36, 823296\n" "buffer_load_dword v155, %0, %1, s36 offen offset:0\n" "buffer_load_dword v157, %0, %1, s36 offen offset:128\n" "buffer_load_dword v159, %0, %1, s36 offen offset:256\n" "s_mov_b32 s36, 835584\n" "buffer_load_dword v160, %0, %1, s36 offen offset:0\n" "buffer_load_dword v162, %0, %1, s36 offen offset:128\n" "buffer_load_dword v164, %0, %1, s36 offen offset:256\n" "s_mov_b32 s36, 860160\n" "buffer_load_dword v161, %0, %1, s36 offen offset:0\n" "buffer_load_dword v163, %0, %1, s36 offen offset:128\n" "buffer_load_dword v165, %0, %1, s36 offen offset:256\n" "s_mov_b32 s36, 847872\n" "buffer_load_dword v166, %0, %1, s36 offen offset:0\n" "buffer_load_dword v168, %0, %1, s36 offen offset:128\n" "buffer_load_dword v170, %0, %1, s36 offen offset:256\n" "s_mov_b32 s36, 872448\n" "buffer_load_dword v167, %0, %1, s36 offen offset:0\n" "buffer_load_dword v169, %0, %1, s36 offen offset:128\n" "buffer_load_dword v171, %0, %1, s36 offen offset:256\n" "s_mov_b32 s36, 884736\n" "buffer_load_dword v172, %0, %1, s36 offen offset:0\n" "buffer_load_dword v174, %0, %1, s36 offen offset:128\n" "buffer_load_dword v176, %0, %1, s36 offen offset:256\n" "s_mov_b32 s36, 909312\n" "buffer_load_dword v173, %0, %1, s36 offen offset:0\n" "buffer_load_dword v175, %0, %1, s36 offen offset:128\n" "buffer_load_dword v177, %0, %1, s36 offen offset:256\n" "s_mov_b32 s36, 897024\n" "buffer_load_dword v178, %0, %1, s36 offen offset:0\n" "buffer_load_dword v180, %0, %1, s36 offen offset:128\n" "buffer_load_dword v182, %0, %1, s36 offen offset:256\n" "s_mov_b32 s36, 921600\n" "buffer_load_dword v179, %0, %1, s36 offen offset:0\n" "buffer_load_dword v181, %0, %1, s36 offen offset:128\n" "buffer_load_dword v183, %0, %1, s36 offen offset:256\n" "s_mov_b32 s36, 933888\n" "buffer_load_dword v184, %0, %1, s36 offen offset:0\n" "buffer_load_dword v186, %0, %1, s36 offen offset:128\n" "buffer_load_dword v188, %0, %1, s36 offen offset:256\n" "s_mov_b32 s36, 958464\n" "buffer_load_dword v185, %0, %1, s36 offen offset:0\n" "buffer_load_dword v187, %0, %1, s36 offen offset:128\n" "buffer_load_dword v189, %0, %1, s36 offen offset:256\n" "s_mov_b32 s36, 946176\n" "buffer_load_dword v190, %0, %1, s36 offen offset:0\n" "buffer_load_dword v192, %0, %1, s36 offen offset:128\n" "buffer_load_dword v194, %0, %1, s36 offen offset:256\n" "s_mov_b32 s36, 970752\n" "buffer_load_dword v191, %0, %1, s36 offen offset:0\n" "buffer_load_dword v193, %0, %1, s36 offen offset:128\n" "buffer_load_dword v195, %0, %1, s36 offen offset:256\n" :: "v"(wq_off), "s"(rq) : A_CLOB, "s36", "memory")
#define LOAD_WO_T() asm volatile(     "s_mov_b32 s36, 7680\n" "buffer_load_dword v130, %0, %1, s36 offen offset:0\n" "buffer_load_dword v132, %0, %1, s36 offen offset:256\n" "buffer_load_dword v134, %0, %1, s36 offen offset:512\n" "buffer_load_dword v136, %0, %1, s36 offen offset:768\n" "buffer_load_dword v138, %0, %1, s36 offen offset:1024\n" "s_mov_b32 s36, 89600\n" "buffer_load_dword v131, %0, %1, s36 offen offset:0\n" "buffer_load_dword v133, %0, %1, s36 offen offset:256\n" "buffer_load_dword v135, %0, %1, s36 offen offset:512\n" "buffer_load_dword v137, %0, %1, s36 offen offset:768\n" "buffer_load_dword v139, %0, %1, s36 offen offset:1024\n" "s_mov_b32 s36, 8960\n" "buffer_load_dword v140, %0, %1, s36 offen offset:0\n" "buffer_load_dword v142, %0, %1, s36 offen offset:256\n" "buffer_load_dword v144, %0, %1, s36 offen offset:512\n" "buffer_load_dword v146, %0, %1, s36 offen offset:768\n" "buffer_load_dword v148, %0, %1, s36 offen offset:1024\n" "s_mov_b32 s36, 90880\n" "buffer_load_dword v141, %0, %1, s36 offen offset:0\n" "buffer_load_dword v143, %0, %1, s36 offen offset:256\n" "buffer_load_dword v145, %0, %1, s36 offen offset:512\n" "buffer_load_dword v147, %0, %1, s36 offen offset:768\n" "buffer_load_dword v149, %0, %1, s36 offen offset:1024\n" "s_mov_b32 s36, 46080\n" "buffer_load_dword v150, %0, %1, s36 offen offset:0\n" "buffer_load_dword v152, %0, %1, s36 offen offset:256\n" "buffer_load_dword v154, %0, %1, s36 offen offset:512\n" "buffer_load_dword v156, %0, %1, s36 offen offset:768\n" "buffer_load_dword v158, %0, %1, s36 offen offset:1024\n" "s_mov_b32 s36, 128000\n" "buffer_load_dword v151, %0, %1, s36 offen offset:0\n" "buffer_load_dword v153, %0, %1, s36 offen offset:256\n" "buffer_load_dword v155, %0, %1, s36 offen offset:512\n" "buffer_load_dword v157, %0, %1, s36 offen offset:768\n" "buffer_load_dword v159, %0, %1, s36 offen offset:1024\n" "s_mov_b32 s36, 47360\n" "buffer_load_dword v160, %0, %1, s36 offen offset:0\n" "buffer_load_dword v162, %0, %1, s36 offen offset:256\n" "buffer_load_dword v164, %0, %1, s36 offen offset:512\n" "buffer_load_dword v166, %0, %1, s36 offen offset:768\n" "buffer_load_dword v168, %0, %1, s36 offen offset:1024\n" "s_mov_b32 s36, 129280\n" "buffer_load_dword v161, %0, %1, s36 offen offset:0\n" "buffer_load_dword v163, %0, %1, s36 offen offset:256\n" "buffer_load_dword v165, %0, %1, s36 offen offset:512\n" "buffer_load_dword v167, %0, %1, s36 offen offset:768\n" "buffer_load_dword v169, %0, %1, s36 offen offset:1024\n" "s_mov_b32 s36, 48640\n" "buffer_load_dword v170, %0, %1, s36 offen offset:0\n" "buffer_load_dword v172, %0, %1, s36 offen offset:256\n" "buffer_load_dword v174, %0, %1, s36 offen offset:512\n" "buffer_load_dword v176, %0, %1, s36 offen offset:768\n" "buffer_load_dword v178, %0, %1, s36 offen offset:1024\n" "s_mov_b32 s36, 130560\n" "buffer_load_dword v171, %0, %1, s36 offen offset:0\n" "buffer_load_dword v173, %0, %1, s36 offen offset:256\n" "buffer_load_dword v175, %0, %1, s36 offen offset:512\n" "buffer_load_dword v177, %0, %1, s36 offen offset:768\n" "buffer_load_dword v179, %0, %1, s36 offen offset:1024\n" "s_mov_b32 s36, 49920\n" "buffer_load_dword v180, %0, %1, s36 offen offset:0\n" "buffer_load_dword v182, %0, %1, s36 offen offset:256\n" "buffer_load_dword v184, %0, %1, s36 offen offset:512\n" "buffer_load_dword v186, %0, %1, s36 offen offset:768\n" "buffer_load_dword v188, %0, %1, s36 offen offset:1024\n" "s_mov_b32 s36, 131840\n" "buffer_load_dword v181, %0, %1, s36 offen offset:0\n" "buffer_load_dword v183, %0, %1, s36 offen offset:256\n" "buffer_load_dword v185, %0, %1, s36 offen offset:512\n" "buffer_load_dword v187, %0, %1, s36 offen offset:768\n" "buffer_load_dword v189, %0, %1, s36 offen offset:1024\n" "s_mov_b32 s36, 163840\n" "buffer_load_dword v190, %0, %1, s36 offen offset:0\n" "buffer_load_dword v192, %0, %1, s36 offen offset:256\n" "buffer_load_dword v194, %0, %1, s36 offen offset:512\n" "buffer_load_dword v196, %0, %1, s36 offen offset:768\n" "buffer_load_dword v198, %0, %1, s36 offen offset:1024\n" "s_mov_b32 s36, 165120\n" "buffer_load_dword v191, %0, %1, s36 offen offset:0\n" "buffer_load_dword v193, %0, %1, s36 offen offset:256\n" "buffer_load_dword v195, %0, %1, s36 offen offset:512\n" "buffer_load_dword v197, %0, %1, s36 offen offset:768\n" "buffer_load_dword v199, %0, %1, s36 offen offset:1024\n" :: "v"(wo_off), "s"(ro) : A_CLOB, "s36", "memory")
#define LOAD_WO_Z() asm volatile(     "s_mov_b32 s36, 166400\n" "buffer_load_dword v48, %0, %1, s36 offen offset:0\n" "buffer_load_dword v50, %0, %1, s36 offen offset:256\n" "buffer_load_dword v52, %0, %1, s36 offen offset:512\n" "buffer_load_dword v54, %0, %1, s36 offen offset:768\n" "buffer_load_dword v56, %0, %1, s36 offen offset:1024\n" "s_mov_b32 s36, 167680\n" "buffer_load_dword v49, %0, %1, s36 offen offset:0\n" "buffer_load_dword v51, %0, %1, s36 offen offset:256\n" "buffer_load_dword v53, %0, %1, s36 offen offset:512\n" "buffer_load_dword v55, %0, %1, s36 offen offset:768\n" "buffer_load_dword v57, %0, %1, s36 offen offset:1024\n" "s_mov_b32 s36, 204800\n" "buffer_load_dword v58, %0, %1, s36 offen offset:0\n" "buffer_load_dword v60, %0, %1, s36 offen offset:256\n" "buffer_load_dword v62, %0, %1, s36 offen offset:512\n" "buffer_load_dword v64, %0, %1, s36 offen offset:768\n" "buffer_load_dword v66, %0, %1, s36 offen offset:1024\n" "s_mov_b32 s36, 206080\n" "buffer_load_dword v59, %0, %1, s36 offen offset:0\n" "buffer_load_dword v61, %0, %1, s36 offen offset:256\n" "buffer_load_dword v63, %0, %1, s36 offen offset:512\n" "buffer_load_dword v65, %0, %1, s36 offen offset:768\n" "buffer_load_dword v67, %0, %1, s36 offen offset:1024\n" "s_mov_b32 s36, 207360\n" "buffer_load_dword v68, %0, %1, s36 offen offset:0\n" "buffer_load_dword v70, %0, %1, s36 offen offset:256\n" "buffer_load_dword v72, %0, %1, s36 offen offset:512\n" "buffer_load_dword v74, %0, %1, s36 offen offset:768\n" "buffer_load_dword v76, %0, %1, s36 offen offset:1024\n" "s_mov_b32 s36, 208640\n" "buffer_load_dword v69, %0, %1, s36 offen offset:0\n" "buffer_load_dword v71, %0, %1, s36 offen offset:256\n" "buffer_load_dword v73, %0, %1, s36 offen offset:512\n" "buffer_load_dword v75, %0, %1, s36 offen offset:768\n" "buffer_load_dword v77, %0, %1, s36 offen offset:1024\n" "s_mov_b32 s36, 168960\n" "buffer_load_dword v78, %0, %1, s36 offen offset:0\n" "buffer_load_dword v80, %0, %1, s36 offen offset:256\n" "buffer_load_dword v82, %0, %1, s36 offen offset:512\n" "buffer_load_dword v84, %0, %1, s36 offen offset:768\n" "buffer_load_dword v86, %0, %1, s36 offen offset:1024\n" "s_mov_b32 s36, 170240\n" "buffer_load_dword v79, %0, %1, s36 offen offset:0\n" "buffer_load_dword v81, %0, %1, s36 offen offset:256\n" "buffer_load_dword v83, %0, %1, s36 offen offset:512\n" "buffer_load_dword v85, %0, %1, s36 offen offset:768\n" "buffer_load_dword v87, %0, %1, s36 offen offset:1024\n" "s_mov_b32 s36, 171520\n" "buffer_load_dword v88, %0, %1, s36 offen offset:0\n" "buffer_load_dword v90, %0, %1, s36 offen offset:256\n" "buffer_load_dword v92, %0, %1, s36 offen offset:512\n" "buffer_load_dword v94, %0, %1, s36 offen offset:768\n" "buffer_load_dword v96, %0, %1, s36 offen offset:1024\n" "s_mov_b32 s36, 172800\n" "buffer_load_dword v89, %0, %1, s36 offen offset:0\n" "buffer_load_dword v91, %0, %1, s36 offen offset:256\n" "buffer_load_dword v93, %0, %1, s36 offen offset:512\n" "buffer_load_dword v95, %0, %1, s36 offen offset:768\n" "buffer_load_dword v97, %0, %1, s36 offen offset:1024\n" "s_mov_b32 s36, 209920\n" "buffer_load_dword v98, %0, %1, s36 offen offset:0\n" "buffer_load_dword v100, %0, %1, s36 offen offset:256\n" "buffer_load_dword v102, %0, %1, s36 offen offset:512\n" "buffer_load_dword v104, %0, %1, s36 offen offset:768\n" "buffer_load_dword v106, %0, %1, s36 offen offset:1024\n" "s_mov_b32 s36, 211200\n" "buffer_load_dword v99, %0, %1, s36 offen offset:0\n" "buffer_load_dword v101, %0, %1, s36 offen offset:256\n" "buffer_load_dword v103, %0, %1, s36 offen offset:512\n" "buffer_load_dword v105, %0, %1, s36 offen offset:768\n" "buffer_load_dword v107, %0, %1, s36 offen offset:1024\n" "s_mov_b32 s36, 212480\n" "buffer_load_dword v108, %0, %1, s36 offen offset:0\n" "buffer_load_dword v110, %0, %1, s36 offen offset:256\n" "buffer_load_dword v112, %0, %1, s36 offen offset:512\n" "buffer_load_dword v114, %0, %1, s36 offen offset:768\n" "buffer_load_dword v116, %0, %1, s36 offen offset:1024\n" "s_mov_b32 s36, 213760\n" "buffer_load_dword v109, %0, %1, s36 offen offset:0\n" "buffer_load_dword v111, %0, %1, s36 offen offset:256\n" "buffer_load_dword v113, %0, %1, s36 offen offset:512\n" "buffer_load_dword v115, %0, %1, s36 offen offset:768\n" "buffer_load_dword v117, %0, %1, s36 offen offset:1024\n" :: "v"(wo_off), "s"(ro) : A_CLOB, "s36", "memory")
  CSTAMP(0);
  auto layernorm = [&](const float* xv, unsigned* xo) {
    REFRESH();
    float s = 0.f;
#pragma unroll
    for (int i = 0; i < 10; ++i) s += xv[i];
    const float m = wave_sum(s) * (1.f / DM);
    float q = 0.f;
#pragma unroll
    for (int i = 0; i < 10; ++i) { const float d = xv[i] - m; q = fmaf(d, d, q); }
    const float r = 1.f / sqrtf(wave_sum(q) * (1.f / DM) + 1e-5f);
#pragma unroll
    for (int i = 0; i < 5; ++i) xo[lane + 64 * i] = f2bf((xv[2 * i] - m) * r) | (f2bf((xv[2 * i + 1] - m) * r) << 16);
  };
  auto swap_pos = [&](unsigned u) -> unsigned {
    const unsigned o = (unsigned)__builtin_amdgcn_mov_dpp((int)u, 0xB1, 0xf, 0xf, true);
    return (lane & 1) ? __builtin_amdgcn_perm(u, o, 0x07060302u) : __builtin_amdgcn_perm(o, u, 0x05040100u);
  };
  auto qproj = [&](unsigned plane, unsigned xoff) {
    REFRESH();
    f32x4 d0, d1, d2;
    asm volatile(     "ds_read_b128 v[118:121], %3 offset:0\n" "ds_read_b128 v[94:97], %6 offset:0\n" "ds_read_b128 v[98:101], %6 offset:4096\n" "ds_read_b128 v[102:105], %6 offset:8192\n" "ds_read_b128 v[122:125], %3 offset:16\n" "ds_read_b128 v[106:109], %6 offset:12288\n" "ds_read_b128 v[110:113], %6 offset:16384\n" "ds_read_b128 v[114:117], %6 offset:20480\n" "s_waitcnt lgkmcnt(7)\n" "v_perm_b32 v126, v118, v118, %4\n" "v_perm_b32 v127, v118, v118, %5\n" "s_nop 1\n" "v_mfma_f32_4x4x4_16b_bf16 %0, a[100:101], v[126:127], 0\n" "v_perm_b32 v128, v119, v119, %4\n" "v_mfma_f32_4x4x4_16b_bf16 %1, a[102:103], v[126:127], 0\n" "v_perm_b32 v129, v119, v119, %5\n" "v_mfma_f32_4x4x4_16b_bf16 %2, a[104:105], v[126:127], 0\n" "s_nop 0\n" "v_mfma_f32_4x4x4_16b_bf16 %0, a[106:107], v[128:129], %0\n" "v_perm_b32 v126, v120, v120, %4\n" "v_mfma_f32_4x4x4_16b_bf16 %1, a[108:109], v[128:129], %1\n" "v_perm_b32 v127, v120, v120, %5\n" "v_mfma_f32_4x4x4_16b_bf16 %2, a[110:111], v[128:129], %2\n" "s_nop 0\n" "v_mfma_f32_4x4x4_16b_bf16 %0, a[112:113], v[126:127], %0\n" "v_perm_b32 v128, v121, v121, %4\n" "v_mfma_f32_4x4x4_16b_bf16 %1, a[114:115], v[126:127], %1\n" "v_perm_b32 v129, v121, v121, %5\n" "v_mfma_f32_4x4x4_16b_bf16 %2, a[116:117], v[126:127], %2\n" "s_nop 0\n" "s_waitcnt lgkmcnt(3)\n" "v_mfma_f32_4x4x4_16b_bf16 %0, a[118:119], v[128:129], %0\n" "v_perm_b32 v126, v122, v122, %4\n" "v_mfma_f32_4x4x4_16b_bf16 %1, a[120:121], v[128:129], %1\n" "v_perm_b32 v127, v122, v122, %5\n" "v_mfma_f32_4x4x4_16b_bf16 %2, a[122:123], v[128:129], %2\n" "s_nop 0\n" "ds_read_b128 v[118:121], %3 offset:32\n" "v_mfma_f32_4x4x4_16b_bf16 %0, a[124:125], v[126:127], %0\n" "v_perm_b32 v128, v123, v123, %4\n" "v_mfma_f32_4x4x4_16b_bf16 %1, a[126:127], v[126:127], %1\n" "v_perm_b32 v129, v123, v123, %5\n" "v_mfma_f32_4x4x4_16b_bf16 %2, a[128:129], v[126:127], %2\n" "s_nop 0\n" "v_mfma_f32_4x4x4_16b_bf16 %0, a[130:131], v[128:129], %0\n" "v_perm_b32 v126, v124, v124, %4\n" "v_mfma_f32_4x4x4_16b_bf16 %1, a[132:133], v[128:129], %1\n" "v_perm_b32 v127, v124, v124, %5\n" "v_mfma_f32_4x4x4_16b_bf16 %2, a[134:135], v[128:129], %2\n" "s_nop 0\n" "v_mfma_f32_4x4x4_16b_bf16 %0, a[136:137], v[126:127], %0\n" "v_perm_b32 v128, v125, v125, %4\n" "v_mfma_f32_4x4x4_16b_bf16 %1, a[138:139], v[126:127], %1\n" "v_perm_b32 v129, v125, v125, %5\n" "v_mfma_f32_4x4x4_16b_bf16 %2, a[140:141], v[126:127], %2\n" "s_nop 0\n" "s_waitcnt lgkmcnt(0)\n" "v_mfma_f32_4x4x4_16b_bf16 %0, a[142:143], v[128:129], %0\n" "v_perm_b32 v126, v118, v118, %4\n" "v_mfma_f32_4x4x4_16b_bf16 %1, a[144:145], v[128:129], %1\n" "v_perm_b32 v127, v118, v118, %5\n" "v_mfma_f32_4x4x4_16b_bf16 %2, a[146:147], v[128:129], %2\n" "s_nop 0\n" "ds_read_b128 v[122:125], %3 offset:48\n" "v_mfma_f32_4x4x4_16b_bf16 %0, a[148:149], v[126:127], %0\n" "v_perm_b32 v128, v119, v119, %4\n" "v_mfma_f32_4x4x4_16b_bf16 %1, a[150:151], v[126:127], %1\n" "v_perm_b32 v129, v119, v119, %5\n" "v_mfma_f32_4x4x4_16b_bf16 %2, a[152:153], v[126:127], %2\n" "s_nop 0\n" "v_mfma_f32_4x4x4_16b_bf16 %0, a[154:155], v[128:129], %0\n" "v_perm_b32 v126, v120, v120, %4\n" "v_mfma_f32_4x4x4_16b_bf16 %1, a[156:157], v[128:129], %1\n" "v_perm_b32 v127, v120, v120, %5\n" "v_mfma_f32_4x4x4_16b_bf16 %2, a[158:159], v[128:129], %2\n" "s_nop 0\n" "v_mfma_f32_4x4x4_16b_bf16 %0, a[160:161], v[126:127], %0\n" "v_perm_b32 v128, v121, v121, %4\n" "v_mfma_f32_4x4x4_16b_bf16 %1, a[162:163], v[126:127], %1\n" "v_perm_b32 v129, v121, v121, %5\n" "v_mfma_f32_4x4x4_16b_bf16 %2, a[164:165], v[126:127], %2\n" "s_nop 0\n" "s_waitcnt lgkmcnt(0)\n" "v_mfma_f32_4x4x4_16b_bf16 %0, a[166:167], v[128:129], %0\n" "v_perm_b32 v126, v122, v122, %4\n" "v_mfma_f32_4x4x4_16b_bf16 %1, a[168:169], v[128:129], %1\n" "v_perm_b32 v127, v122, v122, %5\n" "v_mfma_f32_4x4x4_16b_bf16 %2, a[170:171], v[128:129], %2\n" "s_nop 0\n" "ds_read_b128 v[118:121], %3 offset:64\n" "v_mfma_f32_4x4x4_16b_bf16 %0, a[172:173], v[126:127], %0\n" "v_perm_b32 v128, v123, v123, %4\n" "v_mfma_f32_4x4x4_16b_bf16 %1, a[174:175], v[126:127], %1\n" "v_perm_b32 v129, v123, v123, %5\n" "v_mfma_f32_4x4x4_16b_bf16 %2, a[176:177], v[126:127], %2\n" "s_nop 0\n" "v_mfma_f32_4x4x4_16b_bf16 %0, a[178:179], v[128:129], %0\n" "v_perm_b32 v126, v124, v124, %4\n" "v_mfma_f32_4x4x4_16b_bf16 %1, a[180:181], v[128:129], %1\n" "v_perm_b32 v127, v124, v124, %5\n" "v_mfma_f32_4x4x4_16b_bf16 %2, a[182:183], v[128:129], %2\n" "s_nop 0\n" "v_mfma_f32_4x4x4_16b_bf16 %0, a[184:185], v[126:127], %0\n" "v_perm_b32 v128, v125, v125, %4\n" "v_mfma_f32_4x4x4_16b_bf16 %1, a[186:187], v[126:127], %1\n" "v_perm_b32 v129, v125, v125, %5\n" "v_mfma_f32_4x4x4_16b_bf16 %2, a[188:189], v[126:127], %2\n" "s_nop 0\n" "s_waitcnt lgkmcnt(0)\n" "v_mfma_f32_4x4x4_16b_bf16 %0, a[190:191], v[128:129], %0\n" "v_perm_b32 v126, v118, v118, %4\n" "v_mfma_f32_4x4x4_16b_bf16 %1, a[192:193], v[128:129], %1\n" "v_perm_b32 v127, v118, v118, %5\n" "v_mfma_f32_4x4x4_16b_bf16 %2, a[194:195], v[128:129], %2\n" "s_nop 0\n" "ds_read_b128 v[122:125], %3 offset:80\n" "v_mfma_f32_4x4x4_16b_bf16 %0, a[196:197], v[126:127], %0\n" "v_perm_b32 v128, v119, v119, %4\n" "v_mfma_f32_4x4x4_16b_bf16 %1, a[198:199], v[126:127], %1\n" "v_perm_b32 v129, v119, v119, %5\n" "v_mfma_f32_4x4x4_16b_bf16 %2, a[200:201], v[126:127], %2\n" "s_nop 0\n" "v_mfma_f32_4x4x4_16b_bf16 %0, a[202:203], v[128:129], %0\n" "v_perm_b32 v126, v120, v120, %4\n" "v_mfma_f32_4x4x4_16b_bf16 %1, a[204:205], v[128:129], %1\n" "v_perm_b32 v127, v120, v120, %5\n" "v_mfma_f32_4x4x4_16b_bf16 %2, a[206:207], v[128:129], %2\n" "s_nop 0\n" "v_mfma_f32_4x4x4_16b_bf16 %0, a[208:209], v[126:127], %0\n" "v_perm_b32 v128, v121, v121, %4\n" "v_mfma_f32_4x4x4_16b_bf16 %1, a[210:211], v[126:127], %1\n" "v_perm_b32 v129, v121, v121, %5\n" "v_mfma_f32_4x4x4_16b_bf16 %2, a[212:213], v[126:127], %2\n" "s_nop 0\n" "s_waitcnt lgkmcnt(0)\n" "v_mfma_f32_4x4x4_16b_bf16 %0, a[214:215], v[128:129], %0\n" "v_perm_b32 v126, v122, v122, %4\n" "v_mfma_f32_4x4x4_16b_bf16 %1, a[216:217], v[128:129], %1\n" "v_perm_b32 v127, v122, v122, %5\n" "v_mfma_f32_4x4x4_16b_bf16 %2, a[218:219], v[128:129], %2\n" "s_nop 0\n" "ds_read_b128 v[118:121], %3 offset:96\n" "v_mfma_f32_4x4x4_16b_bf16 %0, a[220:221], v[126:127], %0\n" "v_perm_b32 v128, v123, v123, %4\n" "v_mfma_f32_4x4x4_16b_bf16 %1, a[222:223], v[126:127], %1\n" "v_perm_b32 v129, v123, v123, %5\n" "v_mfma_f32_4x4x4_16b_bf16 %2, a[224:225], v[126:127], %2\n" "s_nop 0\n" "v_mfma_f32_4x4x4_16b_bf16 %0, a[226:227], v[128:129], %0\n" "v_perm_b32 v126, v124, v124, %4\n" "v_mfma_f32_4x4x4_16b_bf16 %1, a[228:229], v[128:129], %1\n" "v_perm_b32 v127, v124, v124, %5\n" "v_mfma_f32_4x4x4_16b_bf16 %2, a[230:231], v[128:129], %2\n" "s_nop 0\n" "v_mfma_f32_4x4x4_16b_bf16 %0, a[232:233], v[126:127], %0\n" "v_perm_b32 v128, v125, v125, %4\n" "v_mfma_f32_4x4x4_16b_bf16 %1, a[234:235], v[126:127], %1\n" "v_perm_b32 v129, v125, v125, %5\n" "v_mfma_f32_4x4x4_16b_bf16 %2, a[236:237], v[126:127], %2\n" "s_nop 0\n" "s_waitcnt lgkmcnt(0)\n" "v_mfma_f32_4x4x4_16b_bf16 %0, a[238:239], v[128:129], %0\n" "v_perm_b32 v126, v118, v118, %4\n" "v_mfma_f32_4x4x4_16b_bf16 %1, a[240:241], v[128:129], %1\n" "v_perm_b32 v127, v118, v118, %5\n" "v_mfma_f32_4x4x4_16b_bf16 %2, a[242:243], v[128:129], %2\n" "s_nop 0\n" "ds_read_b128 v[122:125], %3 offset:112\n" "v_mfma_f32_4x4x4_16b_bf16 %0, a[244:245], v[126:127], %0\n" "v_perm_b32 v128, v119, v119, %4\n" "v_mfma_f32_4x4x4_16b_bf16 %1, a[246:247], v[126:127], %1\n" "v_perm_b32 v129, v119, v119, %5\n" "v_mfma_f32_4x4x4_16b_bf16 %2, a[248:249], v[126:127], %2\n" "s_nop 0\n" "v_mfma_f32_4x4x4_16b_bf16 %0, v[94:95], v[128:129], %0\n" "v_perm_b32 v126, v120, v120, %4\n" "v_mfma_f32_4x4x4_16b_bf16 %1, v[96:97], v[128:129], %1\n" "v_perm_b32 v127, v120, v120, %5\n" "v_mfma_f32_4x4x4_16b_bf16 %2, v[98:99], v[128:129], %2\n" "s_nop 0\n" "v_mfma_f32_4x4x4_16b_bf16 %0, v[100:101], v[126:127], %0\n" "v_perm_b32 v128, v121, v121, %4\n" "v_mfma_f32_4x4x4_16b_bf16 %1, v[102:103], v[126:127], %1\n" "v_perm_b32 v129, v121, v121, %5\n" "v_mfma_f32_4x4x4_16b_bf16 %2, v[104:105], v[126:127], %2\n" "s_nop 0\n" "s_waitcnt lgkmcnt(0)\n" "v_mfma_f32_4x4x4_16b_bf16 %0, v[106:107], v[128:129], %0\n" "v_perm_b32 v126, v122, v122, %4\n" "v_mfma_f32_4x4x4_16b_bf16 %1, v[108:109], v[128:129], %1\n" "v_perm_b32 v127, v122, v122, %5\n" "v_mfma_f32_4x4x4_16b_bf16 %2, v[110:111], v[128:129], %2\n" "s_nop 0\n" "ds_read_b128 v[118:121], %3 offset:128\n" "v_mfma_f32_4x4x4_16b_bf16 %0, v[112:113], v[126:127], %0\n" "v_perm_b32 v128, v123, v123, %4\n" "v_mfma_f32_4x4x4_16b_bf16 %1, v[114:115], v[126:127], %1\n" "v_perm_b32 v129, v123, v123, %5\n" "v_mfma_f32_4x4x4_16b_bf16 %2, v[116:117], v[126:127], %2\n" "s_nop 0\n" "v_mfma_f32_4x4x4_16b_bf16 %0, v[130:131], v[128:129], %0\n" "v_perm_b32 v126, v124, v124, %4\n" "v_mfma_f32_4x4x4_16b_bf16 %1, v[132:133], v[128:129], %1\n" "v_perm_b32 v127, v124, v124, %5\n" "v_mfma_f32_4x4x4_16b_bf16 %2, v[134:135], v[128:129], %2\n" "s_nop 0\n" "v_mfma_f32_4x4x4_16b_bf16 %0, v[136:137], v[126:127], %0\n" "v_perm_b32 v128, v125, v125, %4\n" "v_mfma_f32_4x4x4_16b_bf16 %1, v[138:139], v[126:127], %1\n" "v_perm_b32 v129, v125, v125, %5\n" "v_mfma_f32_4x4x4_16b_bf16 %2, v[140:141], v[126:127], %2\n" "s_nop 0\n" "s_waitcnt lgkmcnt(0)\n" "v_mfma_f32_4x4x4_16b_bf16 %0, v[142:143], v[128:129], %0\n" "v_perm_b32 v126, v118, v118, %4\n" "v_mfma_f32_4x4x4_16b_bf16 %1, v[144:145], v[128:129], %1\n" "v_perm_b32 v127, v118, v118, %5\n" "v_mfma_f32_4x4x4_16b_bf16 %2, v[146:147], v[128:129], %2\n" "s_nop 0\n" "ds_read_b128 v[122:125], %3 offset:144\n" "v_mfma_f32_4x4x4_16b_bf16 %0, v[148:149], v[126:127], %0\n" "v_perm_b32 v128, v119, v119, %4\n" "v_mfma_f32_4x4x4_16b_bf16 %1, v[150:151], v[126:127], %1\n" "v_perm_b32 v129, v119, v119, %5\n" "v_mfma_f32_4x4x4_16b_bf16 %2, v[152:153], v[126:127], %2\n" "s_nop 0\n" "v_mfma_f32_4x4x4_16b_bf16 %0, v[154:155], v[128:129], %0\n" "v_perm_b32 v126, v120, v120, %4\n" "v_mfma_f32_4x4x4_16b_bf16 %1, v[156:157], v[128:129], %1\n" "v_perm_b32 v127, v120, v120, %5\n" "v_mfma_f32_4x4x4_16b_bf16 %2, v[158:159], v[128:129], %2\n" "s_nop 0\n" "v_mfma_f32_4x4x4_16b_bf16 %0, v[160:161], v[126:127], %0\n" "v_perm_b32 v128, v121, v121, %4\n" "v_mfma_f32_4x4x4_16b_bf16 %1, v[162:163], v[126:127], %1\n" "v_perm_b32 v129, v121, v121, %5\n" "v_mfma_f32_4x4x4_16b_bf16 %2, v[164:165], v[126:127], %2\n" "s_nop 0\n" "s_waitcnt lgkmcnt(0)\n" "v_mfma_f32_4x4x4_16b_bf16 %0, v[166:167], v[128:129], %0\n" "v_perm_b32 v126, v122, v122, %4\n" "v_mfma_f32_4x4x4_16b_bf16 %1, v[168:169], v[128:129], %1\n" "v_perm_b32 v127, v122, v122, %5\n" "v_mfma_f32_4x4x4_16b_bf16 %2, v[170:171], v[128:129], %2\n" "s_nop 0\n" "v_mfma_f32_4x4x4_16b_bf16 %0, v[172:173], v[126:127], %0\n" "v_perm_b32 v128, v123, v123, %4\n" "v_mfma_f32_4x4x4_16b_bf16 %1, v[174:175], v[126:127], %1\n" "v_perm_b32 v129, v123, v123, %5\n" "v_mfma_f32_4x4x4_16b_bf16 %2, v[176:177], v[126:127], %2\n" "s_nop 0\n" "v_mfma_f32_4x4x4_16b_bf16 %0, v[178:179], v[128:129], %0\n" "v_perm_b32 v126, v124, v124, %4\n" "v_mfma_f32_4x4x4_16b_bf16 %1, v[180:181], v[128:129], %1\n" "v_perm_b32 v127, v124, v124, %5\n" "v_mfma_f32_4x4x4_16b_bf16 %2, v[182:183], v[128:129], %2\n" "s_nop 0\n" "v_mfma_f32_4x4x4_16b_bf16 %0, v[184:185], v[126:127], %0\n" "v_perm_b32 v128, v125, v125, %4\n" "v_mfma_f32_4x4x4_16b_bf16 %1, v[186:187], v[126:127], %1\n" "v_perm_b32 v129, v125, v125, %5\n" "v_mfma_f32_4x4x4_16b_bf16 %2, v[188:189], v[126:127], %2\n" "s_nop 0\n" "v_mfma_f32_4x4x4_16b_bf16 %0, v[190:191], v[128:129], %0\n" "v_mfma_f32_4x4x4_16b_bf16 %1, v[192:193], v[128:129], %1\n" "v_mfma_f32_4x4x4_16b_bf16 %2, v[194:195], v[128:129], %2\n" "s_nop 0\n" "s_nop 7\n" : "=&v"(d0), "=&v"(d1), "=&v"(d2) : "v"(ldsb + xoff + 160 * hw), "v"(sel0), "v"(sel1), "v"(ldsb + C_WQ + t * 16) : GV_CLOB);
    const f32x4 dd[3] = {d0, d1, d2};
    const int col = 2 * ((l32 & ~3) + 2 * (lane >> 5) + ((lane >> 1) & 1)) + (lane & 1);
#pragma unroll
    for (int c = 0; c < 3; ++c) {
      const float e0 = rs32(dd[c][0], dd[c][2]), e1 = rs32(dd[c][1], dd[c][3]);
      red[w * 192 + 64 * c + col] = ((lane & 2) ? e1 : e0) + dpp<0x4E>((lane & 2) ? e0 : e1);
    }
    __syncthreads();
    if (w == 0) {
      const int l = lane < 48 ? lane : 47;
      f32x4 s = *(const f32x4*)(red + 4 * l);
#pragma unroll
      for (int k = 1; k < 4; ++k) s += *(const f32x4*)(red + k * 192 + 4 * l);
      if (lane < 48) st16(rw, W_Q + plane * Q_PLANE + j * 1024 + lane * 16, v4u{(f2bf(s.x) << 16) | 1u, (f2bf(s.y) << 16) | 1u, (f2bf(s.z) << 16) | 1u, (f2bf(s.w) << 16) | 1u});
    }
    __syncthreads();
  };
  {
    float xv[10];
#pragma unroll
    for (int i = 0; i < 5; ++i) { const unsigned u = swap_pos(((const unsigned*)a.x)[w * 320 + lane + 64 * i]); xv[2 * i] = bflo(u); xv[2 * i + 1] = bfhi(u); }
    asm volatile(     "s_mov_b32 s36, 0\n" "buffer_load_dword a100, %0, %1, s36 offen offset:0\n" "buffer_load_dword a102, %0, %1, s36 offen offset:128\n" "buffer_load_dword a104, %0, %1, s36 offen offset:256\n" "s_mov_b32 s36, 24576\n" "buffer_load_dword a101, %0, %1, s36 offen offset:0\n" "buffer_load_dword a103, %0, %1, s36 offen offset:128\n" "buffer_load_dword a105, %0, %1, s36 offen offset:256\n" "s_mov_b32 s36, 12288\n" "buffer_load_dword a106, %0, %1, s36 offen offset:0\n" "buffer_load_dword a108, %0, %1, s36 offen offset:128\n" "buffer_load_dword a110, %0, %1, s36 offen offset:256\n" "s_mov_b32 s36, 36864\n" "buffer_load_dword a107, %0, %1, s36 offen offset:0\n" "buffer_load_dword a109, %0, %1, s36 offen offset:128\n" "buffer_load_dword a111, %0, %1, s36 offen offset:256\n" "s_mov_b32 s36, 49152\n" "buffer_load_dword a112, %0, %1, s36 offen offset:0\n" "buffer_load_dword a114, %0, %1, s36 offen offset:128\n" "buffer_load_dword a116, %0, %1, s36 offen offset:256\n" "s_mov_b32 s36, 73728\n" "buffer_load_dword a113, %0, %1, s36 offen offset:0\n" "buffer_load_dword a115, %0, %1, s36 offen offset:128\n" "buffer_load_dword a117, %0, %1, s36 offen offset:256\n" "s_mov_b32 s36, 61440\n" "buffer_load_dword a118, %0, %1, s36 offen offset:0\n" "buffer_load_dword a120, %0, %1, s36 offen offset:128\n" "buffer_load_dword a122, %0, %1, s36 offen offset:256\n" "s_mov_b32 s36, 86016\n" "buffer_load_dword a119, %0, %1, s36 offen offset:0\n" "buffer_load_dword a121, %0, %1, s36 offen offset:128\n" "buffer_load_dword a123, %0, %1, s36 offen offset:256\n" "s_mov_b32 s36, 98304\n" "buffer_load_dword a124, %0, %1, s36 offen offset:0\n" "buffer_load_dword a126, %0, %1, s36 offen offset:128\n" "buffer_load_dword a128, %0, %1, s36 offen offset:256\n" "s_mov_b32 s36, 122880\n" "buffer_load_dword a125, %0, %1, s36 offen offset:0\n" "buffer_load_dword a127, %0, %1, s36 offen offset:128\n" "buffer_load_dword a129, %0, %1, s36 offen offset:256\n" "s_mov_b32 s36, 110592\n" "buffer_load_dword a130, %0, %1, s36 offen offset:0\n" "buffer_load_dword a132, %0, %1, s36 offen offset:128\n" "buffer_load_dword a134, %0, %1, s36 offen offset:256\n" "s_mov_b32 s36, 135168\n" "buffer_load_dword a131, %0, %1, s36 offen offset:0\n" "buffer_load_dword a133, %0, %1, s36 offen offset:128\n" "buffer_load_dword a135, %0, %1, s36 offen offset:256\n" "s_mov_b32 s36, 147456\n" "buffer_load_dword a136, %0, %1, s36 offen offset:0\n" "buffer_load_dword a138, %0, %1, s36 offen offset:128\n" "buffer_load_dword a140, %0, %1, s36 offen offset:256\n" "s_mov_b32 s36, 172032\n" "buffer_load_dword a137, %0, %1, s36 offen offset:0\n" "buffer_load_dword a139, %0, %1, s36 offen offset:128\n" "buffer_load_dword a141, %0, %1, s36 offen offset:256\n" "s_mov_b32 s36, 159744\n" "buffer_load_dword a142, %0, %1, s36 offen offset:0\n" "buffer_load_dword a144, %0, %1, s36 offen offset:128\n" "buffer_load_dword a146, %0, %1, s36 offen offset:256\n" "s_mov_b32 s36, 184320\n" "buffer_load_dword a143, %0, %1, s36 offen offset:0\n" "buffer_load_dword a145, %0, %1, s36 offen offset:128\n" "buffer_load_dword a147, %0, %1, s36 offen offset:256\n" "s_mov_b32 s36, 196608\n" "buffer_load_dword a148, %0, %1, s36 offen offset:0\n" "buffer_load_dword a150, %0, %1, s36 offen offset:128\n" "buffer_load_dword a152, %0, %1, s36 offen offset:256\n" "s_mov_b32 s36, 221184\n" "buffer_load_dword a149, %0, %1, s36 offen offset:0\n" "buffer_load_dword a151, %0, %1, s36 offen offset:128\n" "buffer_load_dword a153, %0, %1, s36 offen offset:256\n" "s_mov_b32 s36, 208896\n" "buffer_load_dword a154, %0, %1, s36 offen offset:0\n" "buffer_load_dword a156, %0, %1, s36 offen offset:128\n" "buffer_load_dword a158, %0, %1, s36 offen offset:256\n" "s_mov_b32 s36, 233472\n" "buffer_load_dword a155, %0, %1, s36 offen offset:0\n" "buffer_load_dword a157, %0, %1, s36 offen offset:128\n" "buffer_load_dword a159, %0, %1, s36 offen offset:256\n" "s_mov_b32 s36, 245760\n" "buffer_load_dword a160, %0, %1, s36 offen offset:0\n" "buffer_load_dword a162, %0, %1, s36 offen offset:128\n" "buffer_load_dword a164, %0, %1, s36 offen offset:256\n" "s_mov_b32 s36, 270336\n" "buffer_load_dword a161, %0, %1, s36 offen offset:0\n" "buffer_load_dword a163, %0, %1, s36 offen offset:128\n" "buffer_load_dword a165, %0, %1, s36 offen offset:256\n" "s_mov_b32 s36, 258048\n" "buffer_load_dword a166, %0, %1, s36 offen offset:0\n" "buffer_load_dword a168, %0, %1, s36 offen offset:128\n" "buffer_load_dword a170, %0, %1, s36 offen offset:256\n" "s_mov_b32 s36, 282624\n" "buffer_load_dword a167, %0, %1, s36 offen offset:0\n" "buffer_load_dword a169, %0, %1, s36 offen offset:128\n" "buffer_load_dword a171, %0, %1, s36 offen offset:256\n" "s_mov_b32 s36, 294912\n" "buffer_load_dword a172, %0, %1, s36 offen offset:0\n" "buffer_load_dword a174, %0, %1, s36 offen offset:128\n" "buffer_load_dword a176, %0, %1, s36 offen offset:256\n" "s_mov_b32 s36, 319488\n" "buffer_load_dword a173, %0, %1, s36 offen offset:0\n" "buffer_load_dword a175, %0, %1, s36 offen offset:128\n" "buffer_load_dword a177, %0, %1, s36 offen offset:256\n" "s_mov_b32 s36, 307200\n" "buffer_load_dword a178, %0, %1, s36 offen offset:0\n" "buffer_load_dword a180, %0, %1, s36 offen offset:128\n" "buffer_load_dword a182, %0, %1, s36 offen offset:256\n" "s_mov_b32 s36, 331776\n" "buffer_load_dword a179, %0, %1, s36 offen offset:0\n" "buffer_load_dword a181, %0, %1, s36 offen offset:128\n" "buffer_load_dword a183, %0, %1, s36 offen offset:256\n" "s_mov_b32 s36, 344064\n" "buffer_load_dword a184, %0, %1, s36 offen offset:0\n" "buffer_load_dword a186, %0, %1, s36 offen offset:128\n" "buffer_load_dword a188, %0, %1, s36 offen offset:256\n" "s_mov_b32 s36, 368640\n" "buffer_load_dword a185, %0, %1, s36 offen offset:0\n" "buffer_load_dword a187, %0, %1, s36 offen offset:128\n" "buffer_load_dword a189, %0, %1, s36 offen offset:256\n" "s_mov_b32 s36, 356352\n" "buffer_load_dword a190, %0, %1, s36 offen offset:0\n" "buffer_load_dword a192, %0, %1, s36 offen offset:128\n" "buffer_load_dword a194, %0, %1, s36 offen offset:256\n" "s_mov_b32 s36, 380928\n" "buffer_load_dword a191, %0, %1, s36 offen offset:0\n" "buffer_load_dword a193, %0, %1, s36 offen offset:128\n" "buffer_load_dword a195, %0, %1, s36 offen offset:256\n" "s_mov_b32 s36, 393216\n" "buffer_load_dword a196, %0, %1, s36 offen offset:0\n" "buffer_load_dword a198, %0, %1, s36 offen offset:128\n" "buffer_load_dword a200, %0, %1, s36 offen offset:256\n" "s_mov_b32 s36, 417792\n" "buffer_load_dword a197, %0, %1, s36 offen offset:0\n" "buffer_load_dword a199, %0, %1, s36 offen offset:128\n" "buffer_load_dword a201, %0, %1, s36 offen offset:256\n" "s_mov_b32 s36, 405504\n" "buffer_load_dword a202, %0, %1, s36 offen offset:0\n" "buffer_load_dword a204, %0, %1, s36 offen offset:128\n" "buffer_load_dword a206, %0, %1, s36 offen offset:256\n" "s_mov_b32 s36, 430080\n" "buffer_load_dword a203, %0, %1, s36 offen offset:0\n" "buffer_load_dword a205, %0, %1, s36 offen offset:128\n" "buffer_load_dword a207, %0, %1, s36 offen offset:256\n" "s_mov_b32 s36, 442368\n" "buffer_load_dword a208, %0, %1, s36 offen offset:0\n" "buffer_load_dword a210, %0, %1, s36 offen offset:128\n" "buffer_load_dword a212, %0, %1, s36 offen offset:256\n" "s_mov_b32 s36, 466944\n" "buffer_load_dword a209, %0, %1, s36 offen offset:0\n" "buffer_load_dword a211, %0, %1, s36 offen offset:128\n" "buffer_load_dword a213, %0, %1, s36 offen offset:256\n" "s_mov_b32 s36, 454656\n" "buffer_load_dword a214, %0, %1, s36 offen offset:0\n" "buffer_load_dword a216, %0, %1, s36 offen offset:128\n" "buffer_load_dword a218, %0, %1, s36 offen offset:256\n" "s_mov_b32 s36, 479232\n" "buffer_load_dword a215, %0, %1, s36 offen offset:0\n" "buffer_load_dword a217, %0, %1, s36 offen offset:128\n" "buffer_load_dword a219, %0, %1, s36 offen offset:256\n" "s_mov_b32 s36, 491520\n" "buffer_load_dword a220, %0, %1, s36 offen offset:0\n" "buffer_load_dword a222, %0, %1, s36 offen offset:128\n" "buffer_load_dword a224, %0, %1, s36 offen offset:256\n" "s_mov_b32 s36, 516096\n" "buffer_load_dword a221, %0, %1, s36 offen offset:0\n" "buffer_load_dword a223, %0, %1, s36 offen offset:128\n" "buffer_load_dword a225, %0, %1, s36 offen offset:256\n" "s_mov_b32 s36, 503808\n" "buffer_load_dword a226, %0, %1, s36 offen offset:0\n" "buffer_load_dword a228, %0, %1, s36 offen offset:128\n" "buffer_load_dword a230, %0, %1, s36 offen offset:256\n" "s_mov_b32 s36, 528384\n" "buffer_load_dword a227, %0, %1, s36 offen offset:0\n" "buffer_load_dword a229, %0, %1, s36 offen offset:128\n" "buffer_load_dword a231, %0, %1, s36 offen offset:256\n" "s_mov_b32 s36, 540672\n" "buffer_load_dword a232, %0, %1, s36 offen offset:0\n" "buffer_load_dword a234, %0, %1, s36 offen offset:128\n" "buffer_load_dword a236, %0, %1, s36 offen offset:256\n" "s_mov_b32 s36, 565248\n" "buffer_load_dword a233, %0, %1, s36 offen offset:0\n" "buffer_load_dword a235, %0, %1, s36 offen offset:128\n" "buffer_load_dword a237, %0, %1, s36 offen offset:256\n" "s_mov_b32 s36, 552960\n" "buffer_load_dword a238, %0, %1, s36 offen offset:0\n" "buffer_load_dword a240, %0, %1, s36 offen offset:128\n" "buffer_load_dword a242, %0, %1, s36 offen offset:256\n" "s_mov_b32 s36, 577536\n" "buffer_load_dword a239, %0, %1, s36 offen offset:0\n" "buffer_load_dword a241, %0, %1, s36 offen offset:128\n" "buffer_load_dword a243, %0, %1, s36 offen offset:256\n" "s_mov_b32 s36, 589824\n" "buffer_load_dword a244, %0, %1, s36 offen offset:0\n" "buffer_load_dword a246, %0, %1, s36 offen offset:128\n" "buffer_load_dword a248, %0, %1, s36 offen offset:256\n" "s_mov_b32 s36, 614400\n" "buffer_load_dword a245, %0, %1, s36 offen offset:0\n" "buffer_load_dword a247, %0, %1, s36 offen offset:128\n" "buffer_load_dword a249, %0, %1, s36 offen offset:256\n" :: "v"(wq_off), "s"(rq) : A_CLOB, "s36", "memory");
    layernorm(xv, (unsigned*)(lds + C_RED + 4096 + w * 1280));
  }
  LOAD_WQ_T();
  {
    unsigned tmp[24];
    const rsrc_t rqq = __builtin_amdgcn_make_buffer_rsrc((void*)a.wq, 0, DM * HD * 2, 0x00020000);
#pragma unroll
    for (int p = 0; p < 4; ++p)
#pragma unroll
      for (int c = 0; c < 3; ++c)
#pragma unroll
        for (int h = 0; h < 2; ++h) { const int q = 25 + p, row = (q / 4) * 8 + 4 * ((q >> 1) & 1) + (q & 1) + 2 * h;
          tmp[p * 6 + c * 2 + h] = __builtin_amdgcn_raw_buffer_load_b32(rqq, wq_off + 128 * c, row * HD * 2, 0); }
#pragma unroll
    for (int i = 0; i < 6; ++i) *(v4u*)(lds + C_WQ + i * 4096 + t * 16) = v4u{tmp[4 * i], tmp[4 * i + 1], tmp[4 * i + 2], tmp[4 * i + 3]};
    DRAIN();
  }
  __syncthreads();
  {
    const v4u rg = mk_rsrc(a.wg, DM * DF * 2), ru = mk_rsrc(a.wu, DM * DF * 2);
#define gu_off ((40 * (t >> 4) * (DF / 2) + 24 * j + 3 * (t & 7)) * 4)
    qproj(0, C_RED + 4096);
    CSTAMP(1);
    asm volatile(     "s_mov_b32 exec_lo, 0x00ff00ff\n" "s_mov_b32 exec_hi, 0x00ff00ff\n" "s_mov_b32 s36, 0\n" "buffer_load_dwordx3 a[0:2], %0, %1, s36 offen\n" "s_mov_b32 s36, 3072\n" "buffer_load_dwordx3 a[4:6], %0, %1, s36 offen\n" "s_mov_b32 s36, 6144\n" "buffer_load_dwordx3 a[8:10], %0, %1, s36 offen\n" "s_mov_b32 s36, 9216\n" "buffer_load_dwordx3 a[12:14], %0, %1, s36 offen\n" "s_mov_b32 s36, 12288\n" "buffer_load_dwordx3 a[16:18], %0, %1, s36 offen\n" "s_mov_b32 s36, 15360\n" "buffer_load_dwordx3 a[20:22], %0, %1, s36 offen\n" "s_mov_b32 s36, 18432\n" "buffer_load_dwordx3 a[24:26], %0, %1, s36 offen\n" "s_mov_b32 s36, 21504\n" "buffer_load_dwordx3 a[28:30], %0, %1, s36 offen\n" "s_mov_b32 s36, 24576\n" "buffer_load_dwordx3 a[32:34], %0, %1, s36 offen\n" "s_mov_b32 s36, 27648\n" "buffer_load_dwordx3 a[36:38], %0, %1, s36 offen\n" "s_mov_b32 s36, 30720\n" "buffer_load_dwordx3 a[40:42], %0, %1, s36 offen\n" "s_mov_b32 s36, 33792\n" "buffer_load_dwordx3 a[44:46], %0, %1, s36 offen\n" "s_mov_b32 s36, 36864\n" "buffer_load_dwordx3 a[48:50], %0, %1, s36 offen\n" "s_mov_b32 s36, 39936\n" "buffer_load_dwordx3 a[52:54], %0, %1, s36 offen\n" "s_mov_b32 s36, 43008\n" "buffer_load_dwordx3 a[56:58], %0, %1, s36 offen\n" "s_mov_b32 s36, 46080\n" "buffer_load_dwordx3 a[60:62], %0, %1, s36 offen\n" "s_mov_b32 s36, 49152\n" "buffer_load_dwordx3 a[64:66], %0, %1, s36 offen\n" "s_mov_b32 s36, 52224\n" "buffer_load_dwordx3 a[68:70], %0, %1, s36 offen\n" "s_mov_b32 s36, 55296\n" "buffer_load_dwordx3 a[72:74], %0, %1, s36 offen\n" "s_mov_b32 s36, 58368\n" "buffer_load_dwordx3 a[76:78], %0, %1, s36 offen\n" "s_mov_b32 s36, 61440\n" "buffer_load_dwordx3 a[80:82], %0, %1, s36 offen\n" "s_mov_b32 s36, 64512\n" "buffer_load_dwordx3 a[84:86], %0, %1, s36 offen\n" "s_mov_b32 s36, 67584\n" "buffer_load_dwordx3 a[88:90], %0, %1, s36 offen\n" "s_mov_b32 s36, 70656\n" "buffer_load_dwordx3 a[92:94], %0, %1, s36 offen\n" "s_mov_b32 s36, 73728\n" "buffer_load_dwordx3 a[96:98], %0, %1, s36 offen\n" "s_mov_b32 s36, 76800\n" "buffer_load_dwordx3 v[200:202], %0, %1, s36 offen\n" "s_mov_b32 s36, 79872\n" "buffer_load_dwordx3 v[204:206], %0, %1, s36 offen\n" "s_mov_b32 s36, 82944\n" "buffer_load_dwordx3 v[208:210], %0, %1, s36 offen\n" "s_mov_b32 s36, 86016\n" "buffer_load_dwordx3 v[212:214], %0, %1, s36 offen\n" "s_mov_b32 s36, 89088\n" "buffer_load_dwordx3 v[216:218], %0, %1, s36 offen\n" "s_mov_b32 s36, 92160\n" "buffer_load_dwordx3 v[220:222], %0, %1, s36 offen\n" "s_mov_b32 s36, 95232\n" "buffer_load_dwordx3 v[224:226], %0, %1, s36 offen\n" "s_mov_b32 s36, 98304\n" "buffer_load_dwordx3 v[228:230], %0, %1, s36 offen\n" "s_mov_b32 s36, 101376\n" "buffer_load_dwordx3 v[232:234], %0, %1, s36 offen\n" "s_mov_b32 s36, 104448\n" "buffer_load_dwordx3 v[236:238], %0, %1, s36 offen\n" "s_mov_b32 s36, 107520\n" "buffer_load_dwordx3 v[240:242], %0, %1, s36 offen\n" "s_mov_b32 s36, 110592\n" "buffer_load_dwordx3 v[244:246], %0, %1, s36 offen\n" "s_mov_b32 s36, 113664\n" "buffer_load_dwordx3 v[248:250], %0, %1, s36 offen\n" "s_mov_b32 s36, 116736\n" "buffer_load_dwordx3 v[252:254], %0, %1, s36 offen\n" "s_mov_b32 s36, 119808\n" "buffer_load_dwordx3 v[48:50], %0, %1, s36 offen\n" "s_mov_b64 exec, -1\n" :: "v"(gu_off), "s"(rg), "s"(ru) : A_CLOB, "s36", "memory");
    qproj(1, C_RED + 4096 + 1280);
    asm volatile(     "s_mov_b32 exec_lo, 0xff00ff00\n" "s_mov_b32 exec_hi, 0xff00ff00\n" "s_mov_b32 s36, 0\n" "buffer_load_dwordx3 a[0:2], %0, %2, s36 offen\n" "s_mov_b32 s36, 3072\n" "buffer_load_dwordx3 a[4:6], %0, %2, s36 offen\n" "s_mov_b32 s36, 6144\n" "buffer_load_dwordx3 a[8:10], %0, %2, s36 offen\n" "s_mov_b32 s36, 9216\n" "buffer_load_dwordx3 a[12:14], %0, %2, s36 offen\n" "s_mov_b32 s36, 12288\n" "buffer_load_dwordx3 a[16:18], %0, %2, s36 offen\n" "s_mov_b32 s36, 15360\n" "buffer_load_dwordx3 a[20:22], %0, %2, s36 offen\n" "s_mov_b32 s36, 18432\n" "buffer_load_dwordx3 a[24:26], %0, %2, s36 offen\n" "s_mov_b32 s36, 21504\n" "buffer_load_dwordx3 a[28:30], %0, %2, s36 offen\n" "s_mov_b32 s36, 24576\n" "buffer_load_dwordx3 a[32:34], %0, %2, s36 offen\n" "s_mov_b32 s36, 27648\n" "buffer_load_dwordx3 a[36:38], %0, %2, s36 offen\n" "s_mov_b32 s36, 30720\n" "buffer_load_dwordx3 a[40:42], %0, %2, s36 offen\n" "s_mov_b32 s36, 33792\n" "buffer_load_dwordx3 a[44:46], %0, %2, s36 offen\n" "s_mov_b32 s36, 36864\n" "buffer_load_dwordx3 a[48:50], %0, %2, s36 offen\n" "s_mov_b32 s36, 39936\n" "buffer_load_dwordx3 a[52:54], %0, %2, s36 offen\n" "s_mov_b32 s36, 43008\n" "buffer_load_dwordx3 a[56:58], %0, %2, s36 offen\n" "s_mov_b32 s36, 46080\n" "buffer_load_dwordx3 a[60:62], %0, %2, s36 offen\n" "s_mov_b32 s36, 49152\n" "buffer_load_dwordx3 a[64:66], %0, %2, s36 offen\n" "s_mov_b32 s36, 52224\n" "buffer_load_dwordx3 a[68:70], %0, %2, s36 offen\n" "s_mov_b32 s36, 55296\n" "buffer_load_dwordx3 a[72:74], %0, %2, s36 offen\n" "s_mov_b32 s36, 58368\n" "buffer_load_dwordx3 a[76:78], %0, %2, s36 offen\n" "s_mov_b32 s36, 61440\n" "buffer_load_dwordx3 a[80:82], %0, %2, s36 offen\n" "s_mov_b32 s36, 64512\n" "buffer_load_dwordx3 a[84:86], %0, %2, s36 offen\n" "s_mov_b32 s36, 67584\n" "buffer_load_dwordx3 a[88:90], %0, %2, s36 offen\n" "s_mov_b32 s36, 70656\n" "buffer_load_dwordx3 a[92:94], %0, %2, s36 offen\n" "s_mov_b32 s36, 73728\n" "buffer_load_dwordx3 a[96:98], %0, %2, s36 offen\n" "s_mov_b32 s36, 76800\n" "buffer_load_dwordx3 v[200:202], %0, %2, s36 offen\n" "s_mov_b32 s36, 79872\n" "buffer_load_dwordx3 v[204:206], %0, %2, s36 offen\n" "s_mov_b32 s36, 82944\n" "buffer_load_dwordx3 v[208:210], %0, %2, s36 offen\n" "s_mov_b32 s36, 86016\n" "buffer_load_dwordx3 v[212:214], %0, %2, s36 offen\n" "s_mov_b32 s36, 89088\n" "buffer_load_dwordx3 v[216:218], %0, %2, s36 offen\n" "s_mov_b32 s36, 92160\n" "buffer_load_dwordx3 v[220:222], %0, %2, s36 offen\n" "s_mov_b32 s36, 95232\n" "buffer_load_dwordx3 v[224:226], %0, %2, s36 offen\n" "s_mov_b32 s36, 98304\n" "buffer_load_dwordx3 v[228:230], %0, %2, s36 offen\n" "s_mov_b32 s36, 101376\n" "buffer_load_dwordx3 v[232:234], %0, %2, s36 offen\n" "s_mov_b32 s36, 104448\n" "buffer_load_dwordx3 v[236:238], %0, %2, s36 offen\n" "s_mov_b32 s36, 107520\n" "buffer_load_dwordx3 v[240:242], %0, %2, s36 offen\n" "s_mov_b32 s36, 110592\n" "buffer_load_dwordx3 v[244:246], %0, %2, s36 offen\n" "s_mov_b32 s36, 113664\n" "buffer_load_dwordx3 v[248:250], %0, %2, s36 offen\n" "s_mov_b32 s36, 116736\n" "buffer_load_dwordx3 v[252:254], %0, %2, s36 offen\n" "s_mov_b32 s36, 119808\n" "buffer_load_dwordx3 v[48:50], %0, %2, s36 offen\n" "s_mov_b64 exec, -1\n" :: "v"(gu_off), "s"(rg), "s"(ru) : A_CLOB, "s36", "memory");
  }
#pragma unroll 1
  for (int b = 2; b < 4; ++b) qproj(b, C_RED + 4096 + b * 1280);
  REFRESH();
  LOAD_WO_T();
  asm volatile("s_waitcnt vmcnt(63)\n s_waitcnt vmcnt(62)" ::: "memory");
  asm volatile(     "v_accvgpr_read_b32 v52, a0\n" "v_accvgpr_read_b32 v53, a8\n" "v_accvgpr_read_b32 v54, a1\n" "v_accvgpr_read_b32 v55, a9\n" "ds_write_b128 %0, v[52:55] offset:0\n" "v_accvgpr_read_b32 v56, a2\n" "v_accvgpr_read_b32 v57, a10\n" "v_accvgpr_read_b32 v58, a4\n" "v_accvgpr_read_b32 v59, a12\n" "ds_write_b128 %0, v[56:59] offset:1024\n" "v_accvgpr_read_b32 v60, a5\n" "v_accvgpr_read_b32 v61, a13\n" "v_accvgpr_read_b32 v62, a6\n" "v_accvgpr_read_b32 v63, a14\n" "ds_write_b128 %0, v[60:63] offset:2048\n" "v_accvgpr_read_b32 v64, a16\n" "v_accvgpr_read_b32 v65, a24\n" "v_accvgpr_read_b32 v66, a17\n" "v_accvgpr_read_b32 v67, a25\n" "ds_write_b128 %0, v[64:67] offset:3072\n" "v_accvgpr_read_b32 v68, a18\n" "v_accvgpr_read_b32 v69, a26\n" "v_accvgpr_read_b32 v70, a20\n" "v_accvgpr_read_b32 v71, a28\n" "ds_write_b128 %0, v[68:71] offset:4096\n" "v_accvgpr_read_b32 v72, a21\n" "v_accvgpr_read_b32 v73, a29\n" "v_accvgpr_read_b32 v74, a22\n" "v_accvgpr_read_b32 v75, a30\n" "ds_write_b128 %0, v[72:75] offset:5120\n" "v_accvgpr_read_b32 v76, a32\n" "v_accvgpr_read_b32 v77, a40\n" "v_accvgpr_read_b32 v78, a33\n" "v_accvgpr_read_b32 v79, a41\n" "ds_write_b128 %0, v[76:79] offset:6144\n" "v_accvgpr_read_b32 v80, a34\n" "v_accvgpr_read_b32 v81, a42\n" "v_accvgpr_read_b32 v82, a36\n" "v_accvgpr_read_b32 v83, a44\n" "ds_write_b128 %0, v[80:83] offset:7168\n" "s_waitcnt lgkmcnt(0)\n" "v_accvgpr_read_b32 v52, a37\n" "v_accvgpr_read_b32 v53, a45\n" "v_accvgpr_read_b32 v54, a38\n" "v_accvgpr_read_b32 v55, a46\n" "ds_write_b128 %0, v[52:55] offset:8192\n" "v_accvgpr_read_b32 v56, a48\n" "v_accvgpr_read_b32 v57, a56\n" "v_accvgpr_read_b32 v58, a49\n" "v_accvgpr_read_b32 v59, a57\n" "ds_write_b128 %0, v[56:59] offset:9216\n" "v_accvgpr_read_b32 v60, a50\n" "v_accvgpr_read_b32 v61, a58\n" "v_accvgpr_read_b32 v62, a52\n" "v_accvgpr_read_b32 v63, a60\n" "ds_write_b128 %0, v[60:63] offset:10240\n" "v_accvgpr_read_b32 v64, a53\n" "v_accvgpr_read_b32 v65, a61\n" "v_accvgpr_read_b32 v66, a54\n" "v_accvgpr_read_b32 v67, a62\n" "ds_write_b128 %0, v[64:67] offset:11264\n" "v_accvgpr_read_b32 v68, a64\n" "v_accvgpr_read_b32 v69, a72\n" "v_accvgpr_read_b32 v70, a65\n" "v_accvgpr_read_b32 v71, a73\n" "ds_write_b128 %0, v[68:71] offset:12288\n" "v_accvgpr_read_b32 v72, a66\n" "v_accvgpr_read_b32 v73, a74\n" "v_accvgpr_read_b32 v74, a68\n" "v_accvgpr_read_b32 v75, a76\n" "ds_write_b128 %0, v[72:75] offset:13312\n" "v_accvgpr_read_b32 v76, a69\n" "v_accvgpr_read_b32 v77, a77\n" "v_accvgpr_read_b32 v78, a70\n" "v_accvgpr_read_b32 v79, a78\n" "ds_write_b128 %0, v[76:79] offset:14336\n" "v_accvgpr_read_b32 v80, a80\n" "v_accvgpr_read_b32 v81, a88\n" "v_accvgpr_read_b32 v82, a81\n" "v_accvgpr_read_b32 v83, a89\n" "ds_write_b128 %0, v[80:83] offset:15360\n" "s_waitcnt lgkmcnt(0)\n" "v_accvgpr_read_b32 v52, a82\n" "v_accvgpr_read_b32 v53, a90\n" "v_accvgpr_read_b32 v54, a84\n" "v_accvgpr_read_b32 v55, a92\n" "ds_write_b128 %0, v[52:55] offset:16384\n" "v_accvgpr_read_b32 v56, a85\n" "v_accvgpr_read_b32 v57, a93\n" "v_accvgpr_read_b32 v58, a86\n" "v_accvgpr_read_b32 v59, a94\n" "ds_write_b128 %0, v[56:59] offset:17408\n" "v_accvgpr_read_b32 v60, a96\n" "v_mov_b32 v61, v204\n" "v_accvgpr_read_b32 v62, a97\n" "v_mov_b32 v63, v205\n" "ds_write_b128 %0, v[60:63] offset:18432\n" "v_accvgpr_read_b32 v64, a98\n" "v_mov_b32 v65, v206\n" "v_mov_b32 v66, v200\n" "v_mov_b32 v67, v208\n" "ds_write_b128 %0, v[64:67] offset:19456\n" "v_mov_b32 v68, v201\n" "v_mov_b32 v69, v209\n" "v_mov_b32 v70, v202\n" "v_mov_b32 v71, v210\n" "ds_write_b128 %0, v[68:71] offset:20480\n" "v_mov_b32 v72, v212\n" "v_mov_b32 v73, v220\n" "v_mov_b32 v74, v213\n" "v_mov_b32 v75, v221\n" "ds_write_b128 %0, v[72:75] offset:21504\n" "v_mov_b32 v76, v214\n" "v_mov_b32 v77, v222\n" "v_mov_b32 v78, v216\n" "v_mov_b32 v79, v224\n" "ds_write_b128 %0, v[76:79] offset:22528\n" "v_mov_b32 v80, v217\n" "v_mov_b32 v81, v225\n" "v_mov_b32 v82, v218\n" "v_mov_b32 v83, v226\n" "ds_write_b128 %0, v[80:83] offset:23552\n" "s_waitcnt lgkmcnt(0)\n" "v_mov_b32 v52, v228\n" "v_mov_b32 v53, v236\n" "v_mov_b32 v54, v229\n" "v_mov_b32 v55, v237\n" "ds_write_b128 %0, v[52:55] offset:24576\n" "v_mov_b32 v56, v230\n" "v_mov_b32 v57, v238\n" "v_mov_b32 v58, v232\n" "v_mov_b32 v59, v240\n" "ds_write_b128 %0, v[56:59] offset:25600\n" "v_mov_b32 v60, v233\n" "v_mov_b32 v61, v241\n" "v_mov_b32 v62, v234\n" "v_mov_b32 v63, v242\n" "ds_write_b128 %0, v[60:63] offset:26624\n" "v_mov_b32 v64, v244\n" "v_mov_b32 v65, v252\n" "v_mov_b32 v66, v245\n" "v_mov_b32 v67, v253\n" "ds_write_b128 %0, v[64:67] offset:27648\n" "v_mov_b32 v68, v246\n" "v_mov_b32 v69, v254\n" "v_mov_b32 v70, v248\n" "v_mov_b32 v71, v48\n" "ds_write_b128 %0, v[68:71] offset:28672\n" "v_mov_b32 v72, v249\n" "v_mov_b32 v73, v49\n" "v_mov_b32 v74, v250\n" "v_mov_b32 v75, v50\n" "ds_write_b128 %0, v[72:75] offset:29696\n" "s_waitcnt lgkmcnt(0)\n" :: "v"(ldsb + C_WGU + w * 30720 + lane * 16) : A_CLOB, "memory");
  asm volatile(     "s_mov_b32 s36, 0\n" "buffer_load_dword v200, %0, %1, s36 offen offset:0\n" "buffer_load_dword v202, %0, %1, s36 offen offset:256\n" "buffer_load_dword v204, %0, %1, s36 offen offset:512\n" "buffer_load_dword v206, %0, %1, s36 offen offset:768\n" "buffer_load_dword v208, %0, %1, s36 offen offset:1024\n" "s_mov_b32 s36, 1280\n" "buffer_load_dword v201, %0, %1, s36 offen offset:0\n" "buffer_load_dword v203, %0, %1, s36 offen offset:256\n" "buffer_load_dword v205, %0, %1, s36 offen offset:512\n" "buffer_load_dword v207, %0, %1, s36 offen offset:768\n" "buffer_load_dword v209, %0, %1, s36 offen offset:1024\n" "s_mov_b32 s36, 2560\n" "buffer_load_dword v210, %0, %1, s36 offen offset:0\n" "buffer_load_dword v212, %0, %1, s36 offen offset:256\n" "buffer_load_dword v214, %0, %1, s36 offen offset:512\n" "buffer_load_dword v216, %0, %1, s36 offen offset:768\n" "buffer_load_dword v218, %0, %1, s36 offen offset:1024\n" "s_mov_b32 s36, 3840\n" "buffer_load_dword v211, %0, %1, s36 offen offset:0\n" "buffer_load_dword v213, %0, %1, s36 offen offset:256\n" "buffer_load_dword v215, %0, %1, s36 offen offset:512\n" "buffer_load_dword v217, %0, %1, s36 offen offset:768\n" "buffer_load_dword v219, %0, %1, s36 offen offset:1024\n" "s_mov_b32 s36, 5120\n" "buffer_load_dword v220, %0, %1, s36 offen offset:0\n" "buffer_load_dword v222, %0, %1, s36 offen offset:256\n" "buffer_load_dword v224, %0, %1, s36 offen offset:512\n" "buffer_load_dword v226, %0, %1, s36 offen offset:768\n" "buffer_load_dword v228, %0, %1, s36 offen offset:1024\n" "s_mov_b32 s36, 6400\n" "buffer_load_dword v221, %0, %1, s36 offen offset:0\n" "buffer_load_dword v223, %0, %1, s36 offen offset:256\n" "buffer_load_dword v225, %0, %1, s36 offen offset:512\n" "buffer_load_dword v227, %0, %1, s36 offen offset:768\n" "buffer_load_dword v229, %0, %1, s36 offen offset:1024\n" "s_mov_b32 s36, 7680\n" "buffer_load_dword v230, %0, %1, s36 offen offset:0\n" "buffer_load_dword v232, %0, %1, s36 offen offset:256\n" "buffer_load_dword v234, %0, %1, s36 offen offset:512\n" "buffer_load_dword v236, %0, %1, s36 offen offset:768\n" "buffer_load_dword v238, %0, %1, s36 offen offset:1024\n" "s_mov_b32 s36, 8960\n" "buffer_load_dword v231, %0, %1, s36 offen offset:0\n" "buffer_load_dword v233, %0, %1, s36 offen offset:256\n" "buffer_load_dword v235, %0, %1, s36 offen offset:512\n" "buffer_load_dword v237, %0, %1, s36 offen offset:768\n" "buffer_load_dword v239, %0, %1, s36 offen offset:1024\n" "s_mov_b32 s36, 10240\n" "buffer_load_dword v240, %0, %1, s36 offen offset:0\n" "buffer_load_dword v242, %0, %1, s36 offen offset:256\n" "buffer_load_dword v244, %0, %1, s36 offen offset:512\n" "buffer_load_dword v246, %0, %1, s36 offen offset:768\n" "buffer_load_dword v248, %0, %1, s36 offen offset:1024\n" "s_mov_b32 s36, 11520\n" "buffer_load_dword v241, %0, %1, s36 offen offset:0\n" "buffer_load_dword v243, %0, %1, s36 offen offset:256\n" "buffer_load_dword v245, %0, %1, s36 offen offset:512\n" "buffer_load_dword v247, %0, %1, s36 offen offset:768\n" "buffer_load_dword v249, %0, %1, s36 offen offset:1024\n" "s_mov_b32 s36, 12800\n" "buffer_load_dword v250, %0, %1, s36 offen offset:0\n" "buffer_load_dword v252, %0, %1, s36 offen offset:256\n" "buffer_load_dword v254, %0, %1, s36 offen offset:512\n" "buffer_load_dword a250, %0, %1, s36 offen offset:768\n" "buffer_load_dword a252, %0, %1, s36 offen offset:1024\n" "s_mov_b32 s36, 14080\n" "buffer_load_dword v251, %0, %1, s36 offen offset:0\n" "buffer_load_dword v253, %0, %1, s36 offen offset:256\n" "buffer_load_dword v255, %0, %1, s36 offen offset:512\n" "buffer_load_dword a251, %0, %1, s36 offen offset:768\n" "buffer_load_dword a253, %0, %1, s36 offen offset:1024\n" :: "v"(wd_off), "s"(rd) : A_CLOB, "s36", "memory");
  asm volatile(     "s_mov_b32 s36, 0\n" "buffer_load_dword a0, %0, %1, s36 offen offset:0\n" "buffer_load_dword a2, %0, %1, s36 offen offset:256\n" "buffer_load_dword a4, %0, %1, s36 offen offset:512\n" "buffer_load_dword a6, %0, %1, s36 offen offset:768\n" "buffer_load_dword a8, %0, %1, s36 offen offset:1024\n" "s_mov_b32 s36, 81920\n" "buffer_load_dword a1, %0, %1, s36 offen offset:0\n" "buffer_load_dword a3, %0, %1, s36 offen offset:256\n" "buffer_load_dword a5, %0, %1, s36 offen offset:512\n" "buffer_load_dword a7, %0, %1, s36 offen offset:768\n" "buffer_load_dword a9, %0, %1, s36 offen offset:1024\n" "s_mov_b32 s36, 1280\n" "buffer_load_dword a10, %0, %1, s36 offen offset:0\n" "buffer_load_dword a12, %0, %1, s36 offen offset:256\n" "buffer_load_dword a14, %0, %1, s36 offen offset:512\n" "buffer_load_dword a16, %0, %1, s36 offen offset:768\n" "buffer_load_dword a18, %0, %1, s36 offen offset:1024\n" "s_mov_b32 s36, 83200\n" "buffer_load_dword a11, %0, %1, s36 offen offset:0\n" "buffer_load_dword a13, %0, %1, s36 offen offset:256\n" "buffer_load_dword a15, %0, %1, s36 offen offset:512\n" "buffer_load_dword a17, %0, %1, s36 offen offset:768\n" "buffer_load_dword a19, %0, %1, s36 offen offset:1024\n" "s_mov_b32 s36, 2560\n" "buffer_load_dword a20, %0, %1, s36 offen offset:0\n" "buffer_load_dword a22, %0, %1, s36 offen offset:256\n" "buffer_load_dword a24, %0, %1, s36 offen offset:512\n" "buffer_load_dword a26, %0, %1, s36 offen offset:768\n" "buffer_load_dword a28, %0, %1, s36 offen offset:1024\n" "s_mov_b32 s36, 84480\n" "buffer_load_dword a21, %0, %1, s36 offen offset:0\n" "buffer_load_dword a23, %0, %1, s36 offen offset:256\n" "buffer_load_dword a25, %0, %1, s36 offen offset:512\n" "buffer_load_dword a27, %0, %1, s36 offen offset:768\n" "buffer_load_dword a29, %0, %1, s36 offen offset:1024\n" "s_mov_b32 s36, 3840\n" "buffer_load_dword a30, %0, %1, s36 offen offset:0\n" "buffer_load_dword a32, %0, %1, s36 offen offset:256\n" "buffer_load_dword a34, %0, %1, s36 offen offset:512\n" "buffer_load_dword a36, %0, %1, s36 offen offset:768\n" "buffer_load_dword a38, %0, %1, s36 offen offset:1024\n" "s_mov_b32 s36, 85760\n" "buffer_load_dword a31, %0, %1, s36 offen offset:0\n" "buffer_load_dword a33, %0, %1, s36 offen offset:256\n" "buffer_load_dword a35, %0, %1, s36 offen offset:512\n" "buffer_load_dword a37, %0, %1, s36 offen offset:768\n" "buffer_load_dword a39, %0, %1, s36 offen offset:1024\n" "s_mov_b32 s36, 40960\n" "buffer_load_dword a40, %0, %1, s36 offen offset:0\n" "buffer_load_dword a42, %0, %1, s36 offen offset:256\n" "buffer_load_dword a44, %0, %1, s36 offen offset:512\n" "buffer_load_dword a46, %0, %1, s36 offen offset:768\n" "buffer_load_dword a48, %0, %1, s36 offen offset:1024\n" "s_mov_b32 s36, 122880\n" "buffer_load_dword a41, %0, %1, s36 offen offset:0\n" "buffer_load_dword a43, %0, %1, s36 offen offset:256\n" "buffer_load_dword a45, %0, %1, s36 offen offset:512\n" "buffer_load_dword a47, %0, %1, s36 offen offset:768\n" "buffer_load_dword a49, %0, %1, s36 offen offset:1024\n" "s_mov_b32 s36, 42240\n" "buffer_load_dword a50, %0, %1, s36 offen offset:0\n" "buffer_load_dword a52, %0, %1, s36 offen offset:256\n" "buffer_load_dword a54, %0, %1, s36 offen offset:512\n" "buffer_load_dword a56, %0, %1, s36 offen offset:768\n" "buffer_load_dword a58, %0, %1, s36 offen offset:1024\n" "s_mov_b32 s36, 124160\n" "buffer_load_dword a51, %0, %1, s36 offen offset:0\n" "buffer_load_dword a53, %0, %1, s36 offen offset:256\n" "buffer_load_dword a55, %0, %1, s36 offen offset:512\n" "buffer_load_dword a57, %0, %1, s36 offen offset:768\n" "buffer_load_dword a59, %0, %1, s36 offen offset:1024\n" "s_mov_b32 s36, 43520\n" "buffer_load_dword a60, %0, %1, s36 offen offset:0\n" "buffer_load_dword a62, %0, %1, s36 offen offset:256\n" "buffer_load_dword a64, %0, %1, s36 offen offset:512\n" "buffer_load_dword a66, %0, %1, s36 offen offset:768\n" "buffer_load_dword a68, %0, %1, s36 offen offset:1024\n" "s_mov_b32 s36, 125440\n" "buffer_load_dword a61, %0, %1, s36 offen offset:0\n" "buffer_load_dword a63, %0, %1, s36 offen offset:256\n" "buffer_load_dword a65, %0, %1, s36 offen offset:512\n" "buffer_load_dword a67, %0, %1, s36 offen offset:768\n" "buffer_load_dword a69, %0, %1, s36 offen offset:1024\n" "s_mov_b32 s36, 44800\n" "buffer_load_dword a70, %0, %1, s36 offen offset:0\n" "buffer_load_dword a72, %0, %1, s36 offen offset:256\n" "buffer_load_dword a74, %0, %1, s36 offen offset:512\n" "buffer_load_dword a76, %0, %1, s36 offen offset:768\n" "buffer_load_dword a78, %0, %1, s36 offen offset:1024\n" "s_mov_b32 s36, 126720\n" "buffer_load_dword a71, %0, %1, s36 offen offset:0\n" "buffer_load_dword a73, %0, %1, s36 offen offset:256\n" "buffer_load_dword a75, %0, %1, s36 offen offset:512\n" "buffer_load_dword a77, %0, %1, s36 offen offset:768\n" "buffer_load_dword a79, %0, %1, s36 offen offset:1024\n" "s_mov_b32 s36, 5120\n" "buffer_load_dword a80, %0, %1, s36 offen offset:0\n" "buffer_load_dword a82, %0, %1, s36 offen offset:256\n" "buffer_load_dword a84, %0, %1, s36 offen offset:512\n" "buffer_load_dword a86, %0, %1, s36 offen offset:768\n" "buffer_load_dword a88, %0, %1, s36 offen offset:1024\n" "s_mov_b32 s36, 87040\n" "buffer_load_dword a81, %0, %1, s36 offen offset:0\n" "buffer_load_dword a83, %0, %1, s36 offen offset:256\n" "buffer_load_dword a85, %0, %1, s36 offen offset:512\n" "buffer_load_dword a87, %0, %1, s36 offen offset:768\n" "buffer_load_dword a89, %0, %1, s36 offen offset:1024\n" "s_mov_b32 s36, 6400\n" "buffer_load_dword a90, %0, %1, s36 offen offset:0\n" "buffer_load_dword a92, %0, %1, s36 offen offset:256\n" "buffer_load_dword a94, %0, %1, s36 offen offset:512\n" "buffer_load_dword a96, %0, %1, s36 offen offset:768\n" "buffer_load_dword a98, %0, %1, s36 offen offset:1024\n" "s_mov_b32 s36, 88320\n" "buffer_load_dword a91, %0, %1, s36 offen offset:0\n" "buffer_load_dword a93, %0, %1, s36 offen offset:256\n" "buffer_load_dword a95, %0, %1, s36 offen offset:512\n" "buffer_load_dword a97, %0, %1, s36 offen offset:768\n" "buffer_load_dword a99, %0, %1, s36 offen offset:1024\n" :: "v"(wo_off), "s"(ro) : A_CLOB, "s36", "memory");
  LOAD_WO_Z();
  DRAIN();
  __syncthreads();
  CSTAMP(2);

  auto publish640 = [&](const f32x4* dd, unsigned wbase, unsigned tag) {
    REFRESH();
#pragma unroll
    for (int c = 0; c < 5; ++c) {
      float v[4];
#pragma unroll
      for (int i = 0; i < 4; ++i) v[i] = dd[c][i] + dpp<0x4E>(dd[c][i]);
      *(float2*)(red + w * 640 + 2 * (lane + 64 * c)) = (lane & 2) ? make_float2(v[2], v[3]) : make_float2(v[0], v[1]);
    }
    __syncthreads();
    if (t < 160) {
      f32x4 s = *(const f32x4*)(red + 4 * t);
#pragma unroll
      for (int k = 1; k < 4; ++k) s += *(const f32x4*)(red + k * 640 + 4 * t);
      const v4u ws_ = v4u{tagw(s.x, tag), tagw(s.y, tag), tagw(s.z, tag), tagw(s.w, tag)};
      st16(rw, wbase + j * 2560 + t * 16, ws_);
      *(v4u*)(red + 4 * t) = ws_;
    }
  };
  auto resid_ln = [&](const unsigned* xold, unsigned* xnew, bool ln) {
    REFRESH();
    float* st = (float*)(lds + C_ST);
    float x[4] = {0.f, 0.f, 0.f, 0.f}, s1 = 0.f, s2 = 0.f;
    const float piv = bflo(xold[0]);
    if (t < 160) {
      const f32x4 s = (*(const f32x4*)(red + 4 * t) + *(const f32x4*)(red + 640 + 4 * t)) + (*(const f32x4*)(red + 1280 + 4 * t) + *(const f32x4*)(red + 1920 + 4 * t));
      const v2u u = *(const v2u*)(xold + 2 * t);
      const unsigned p0 = pk2(bflo(u.x) + rbf(s.x), bfhi(u.x) + rbf(s.y)), p1 = pk2(bflo(u.y) + rbf(s.z), bfhi(u.y) + rbf(s.w));
      *(v2u*)(xnew + 2 * t) = v2u{p0, p1};
      x[0] = bflo(p0) - piv; x[1] = bfhi(p0) - piv; x[2] = bflo(p1) - piv; x[3] = bfhi(p1) - piv;
      s1 = (x[0] + x[1]) + (x[2] + x[3]);
      s2 = fmaf(x[0], x[0], x[1] * x[1]) + fmaf(x[2], x[2], x[3] * x[3]);
    }
    if (!ln) { __syncthreads(); return; }
    s1 = wave_sum(s1); s2 = wave_sum(s2);
    if (lane == 0) *(float2*)(st + 2 * w) = make_float2(s1, s2);
    __syncthreads();
    const f32x4 a0 = *(const f32x4*)st; const float2 a1 = *(const float2*)(st + 4);
    const float m = ((a0.x + a0.z) + a1.x) * (1.f / DM);
    const float var = fmaxf(((a0.y + a0.w) + a1.y) * (1.f / DM) - m * m, 0.f);
    const float r = __builtin_amdgcn_rsqf(var + 1e-5f);
    if (t < 160) *(v2u*)(xn + 2 * t) = v2u{pk2((x[0] - m) * r, (x[1] - m) * r), pk2((x[2] - m) * r, (x[3] - m) * r)};
    __syncthreads();
  };
#define GATHER(TXT, wbase, tag, woff, wrs) do { REFRESH(); \
    asm volatile("s_waitcnt lgkmcnt(0)\n" TXT :: "v"((wbase) + hw * 4 * 2560 + l32 * 16), "s"(rw4), "s"(tag), "v"(ldsb + C_RED + w * 2560 + l32 * 16 + (lane >> 5) * 8), "v"(woff), "s"(wrs), "v"((wbase) + (w * 8 + (lane & 7)) * 2560 + 2556), "s"((j >> 3) == w ? (j & 7) : 8), "v"(ldsb + C_RED + l32 * 16) : GV_CLOB); } while (0)

#pragma unroll 1
  for (int n = 0; n < 8; ++n) {
    const int ps = (0xD4 >> n) & 1;
    const int b = (0xED84 >> (2 * n)) & 3;
    const unsigned tag = b + 1, ytag = 1u << (n & 1);
    const unsigned pbase = W_PART + (ps * 2 + (b & 1)) * PART_PLANE + j * PART_HEAD;
    float one = 1.f;
    REFRESH();
    if (ps == 0 && w == 0) {
#pragma unroll
      for (int i = 0; i < 5; ++i) xs[(b & 1) * 320 + lane + 64 * i] = swap_pos(((const unsigned*)a.x)[b * 320 + lane + 64 * i]);
    }
    float M, L, O0, O1, O2;
    {
      REFRESH();
      const unsigned ad = pbase + (lane >> 4) * PART_SLOT + (16 * w + (lane & 15)) * 16;
      v4u A[2];
      unsigned spins = 0;
      for (;;) {
        asm volatile("" ::: "memory");
        A[0] = ld16(rw, ad); A[1] = ld16(rw, ad + 4 * PART_SLOT);
        const unsigned bad = (A[0].x ^ tag) | (A[0].y ^ tag) | (A[0].z ^ tag) | (A[0].w ^ tag) | (A[1].x ^ tag) | (A[1].y ^ tag) | (A[1].z ^ tag) | (A[1].w ^ tag);
        if (__all((bad & 15u) == 0u)) break;
        if (spin_fail(spins, rw, 2000 + n)) break;
      }
      const float z0 = __uint_as_float(A[0].w & ~15u), o0 = dpp<0xB1>(z0), z1 = __uint_as_float(A[1].w & ~15u), o1 = dpp<0xB1>(z1);
      const float m0 = (lane & 1) ? o0 : z0, l0 = (lane & 1) ? z0 : o0, m1 = (lane & 1) ? o1 : z1, l1 = (lane & 1) ? z1 : o1;
      M = x32max(x16max(fmaxf(m0, m1)));
      const float c0 = __builtin_amdgcn_exp2f(m0 - M), c1 = __builtin_amdgcn_exp2f(m1 - M);
      L = l0 * c0 + l1 * c1;
      O0 = __uint_as_float(A[0].x) * c0 + __uint_as_float(A[1].x) * c1;
      O1 = __uint_as_float(A[0].y) * c0 + __uint_as_float(A[1].y) * c1;
      O2 = __uint_as_float(A[0].z) * c0 + __uint_as_float(A[1].z) * c1;
    }
    CSTAMP(4 + 6 * n);
    REFRESH();
    f32x4 dd[5];
    {
      const float inv = __builtin_amdgcn_rcpf(x32sum(x16sum(L)));
      const unsigned o0 = f2bf(x32sum(x16sum(O0)) * inv), o1 = f2bf(x32sum(x16sum(O1)) * inv), o2 = f2bf(x32sum(x16sum(O2)) * inv);
      const int src = 2 * (lane & 7);
      const unsigned ta = (unsigned)__builtin_amdgcn_ds_bpermute(4 * src, (int)o2), tb = (unsigned)__builtin_amdgcn_ds_bpermute(4 * src + 4, (int)o2);
      const unsigned pr = lane < 16 ? (o0 | (o1 << 16)) : (ta | (tb << 16));
      asm volatile(     "s_nop 0\n" "v_readlane_b32 s36, %5, 0\n" "v_readlane_b32 s37, %5, 1\n" "s_nop 0\n" "v_perm_b32 v126, s36, s36, %6\n" "v_perm_b32 v127, s36, s36, %7\n" "s_nop 1\n" "v_mfma_f32_4x4x4_16b_bf16 %0, a[0:1], v[126:127], 0\n" "v_readlane_b32 s38, %5, 2\n" "v_mfma_f32_4x4x4_16b_bf16 %1, a[2:3], v[126:127], 0\n" "v_perm_b32 v128, s37, s37, %6\n" "v_mfma_f32_4x4x4_16b_bf16 %2, a[4:5], v[126:127], 0\n" "v_perm_b32 v129, s37, s37, %7\n" "v_mfma_f32_4x4x4_16b_bf16 %3, a[6:7], v[126:127], 0\n" "v_mfma_f32_4x4x4_16b_bf16 %4, a[8:9], v[126:127], 0\n" "v_mfma_f32_4x4x4_16b_bf16 %0, a[10:11], v[128:129], %0\n" "v_readlane_b32 s39, %5, 3\n" "v_mfma_f32_4x4x4_16b_bf16 %1, a[12:13], v[128:129], %1\n" "v_perm_b32 v126, s38, s38, %6\n" "v_mfma_f32_4x4x4_16b_bf16 %2, a[14:15], v[128:129], %2\n" "v_perm_b32 v127, s38, s38, %7\n" "v_mfma_f32_4x4x4_16b_bf16 %3, a[16:17], v[128:129], %3\n" "v_mfma_f32_4x4x4_16b_bf16 %4, a[18:19], v[128:129], %4\n" "v_mfma_f32_4x4x4_16b_bf16 %0, a[20:21], v[126:127], %0\n" "v_readlane_b32 s36, %5, 4\n" "v_mfma_f32_4x4x4_16b_bf16 %1, a[22:23], v[126:127], %1\n" "v_perm_b32 v128, s39, s39, %6\n" "v_mfma_f32_4x4x4_16b_bf16 %2, a[24:25], v[126:127], %2\n" "v_perm_b32 v129, s39, s39, %7\n" "v_mfma_f32_4x4x4_16b_bf16 %3, a[26:27], v[126:127], %3\n" "v_mfma_f32_4x4x4_16b_bf16 %4, a[28:29], v[126:127], %4\n" "v_mfma_f32_4x4x4_16b_bf16 %0, a[30:31], v[128:129], %0\n" "v_readlane_b32 s37, %5, 5\n" "v_mfma_f32_4x4x4_16b_bf16 %1, a[32:33], v[128:129], %1\n" "v_perm_b32 v126, s36, s36, %6\n" "v_mfma_f32_4x4x4_16b_bf16 %2, a[34:35], v[128:129], %2\n" "v_perm_b32 v127, s36, s36, %7\n" "v_mfma_f32_4x4x4_16b_bf16 %3, a[36:37], v[128:129], %3\n" "v_mfma_f32_4x4x4_16b_bf16 %4, a[38:39], v[128:129], %4\n" "v_mfma_f32_4x4x4_16b_bf16 %0, a[40:41], v[126:127], %0\n" "v_readlane_b32 s38, %5, 6\n" "v_mfma_f32_4x4x4_16b_bf16 %1, a[42:43], v[126:127], %1\n" "v_perm_b32 v128, s37, s37, %6\n" "v_mfma_f32_4x4x4_16b_bf16 %2, a[44:45], v[126:127], %2\n" "v_perm_b32 v129, s37, s37, %7\n" "v_mfma_f32_4x4x4_16b_bf16 %3, a[46:47], v[126:127], %3\n" "v_mfma_f32_4x4x4_16b_bf16 %4, a[48:49], v[126:127], %4\n" "v_mfma_f32_4x4x4_16b_bf16 %0, a[50:51], v[128:129], %0\n" "v_readlane_b32 s39, %5, 7\n" "v_mfma_f32_4x4x4_16b_bf16 %1, a[52:53], v[128:129], %1\n" "v_perm_b32 v126, s38, s38, %6\n" "v_mfma_f32_4x4x4_16b_bf16 %2, a[54:55], v[128:129], %2\n" "v_perm_b32 v127, s38, s38, %7\n" "v_mfma_f32_4x4x4_16b_bf16 %3, a[56:57], v[128:129], %3\n" "v_mfma_f32_4x4x4_16b_bf16 %4, a[58:59], v[128:129], %4\n" "v_mfma_f32_4x4x4_16b_bf16 %0, a[60:61], v[126:127], %0\n" "v_readlane_b32 s36, %5, 8\n" "v_mfma_f32_4x4x4_16b_bf16 %1, a[62:63], v[126:127], %1\n" "v_perm_b32 v128, s39, s39, %6\n" "v_mfma_f32_4x4x4_16b_bf16 %2, a[64:65], v[126:127], %2\n" "v_perm_b32 v129, s39, s39, %7\n" "v_mfma_f32_4x4x4_16b_bf16 %3, a[66:67], v[126:127], %3\n" "v_mfma_f32_4x4x4_16b_bf16 %4, a[68:69], v[126:127], %4\n" "v_mfma_f32_4x4x4_16b_bf16 %0, a[70:71], v[128:129], %0\n" "v_readlane_b32 s37, %5, 9\n" "v_mfma_f32_4x4x4_16b_bf16 %1, a[72:73], v[128:129], %1\n" "v_perm_b32 v126, s36, s36, %6\n" "v_mfma_f32_4x4x4_16b_bf16 %2, a[74:75], v[128:129], %2\n" "v_perm_b32 v127, s36, s36, %7\n" "v_mfma_f32_4x4x4_16b_bf16 %3, a[76:77], v[128:129], %3\n" "v_mfma_f32_4x4x4_16b_bf16 %4, a[78:79], v[128:129], %4\n" "v_mfma_f32_4x4x4_16b_bf16 %0, a[80:81], v[126:127], %0\n" "v_readlane_b32 s38, %5, 10\n" "v_mfma_f32_4x4x4_16b_bf16 %1, a[82:83], v[126:127], %1\n" "v_perm_b32 v128, s37, s37, %6\n" "v_mfma_f32_4x4x4_16b_bf16 %2, a[84:85], v[126:127], %2\n" "v_perm_b32 v129, s37, s37, %7\n" "v_mfma_f32_4x4x4_16b_bf16 %3, a[86:87], v[126:127], %3\n" "v_mfma_f32_4x4x4_16b_bf16 %4, a[88:89], v[126:127], %4\n" "v_mfma_f32_4x4x4_16b_bf16 %0, a[90:91], v[128:129], %0\n" "v_readlane_b32 s39, %5, 11\n" "v_mfma_f32_4x4x4_16b_bf16 %1, a[92:93], v[128:129], %1\n" "v_perm_b32 v126, s38, s38, %6\n" "v_mfma_f32_4x4x4_16b_bf16 %2, a[94:95], v[128:129], %2\n" "v_perm_b32 v127, s38, s38, %7\n" "v_mfma_f32_4x4x4_16b_bf16 %3, a[96:97], v[128:129], %3\n" "v_mfma_f32_4x4x4_16b_bf16 %4, a[98:99], v[128:129], %4\n" "s_waitcnt vmcnt(0)\n" "v_mfma_f32_4x4x4_16b_bf16 %0, v[130:131], v[126:127], %0\n" "v_readlane_b32 s36, %5, 12\n" "v_mfma_f32_4x4x4_16b_bf16 %1, v[132:133], v[126:127], %1\n" "v_perm_b32 v128, s39, s39, %6\n" "v_mfma_f32_4x4x4_16b_bf16 %2, v[134:135], v[126:127], %2\n" "v_perm_b32 v129, s39, s39, %7\n" "v_mfma_f32_4x4x4_16b_bf16 %3, v[136:137], v[126:127], %3\n" "v_mfma_f32_4x4x4_16b_bf16 %4, v[138:139], v[126:127], %4\n" "v_mfma_f32_4x4x4_16b_bf16 %0, v[140:141], v[128:129], %0\n" "v_readlane_b32 s37, %5, 13\n" "v_mfma_f32_4x4x4_16b_bf16 %1, v[142:143], v[128:129], %1\n" "v_perm_b32 v126, s36, s36, %6\n" "v_mfma_f32_4x4x4_16b_bf16 %2, v[144:145], v[128:129], %2\n" "v_perm_b32 v127, s36, s36, %7\n" "v_mfma_f32_4x4x4_16b_bf16 %3, v[146:147], v[128:129], %3\n" "v_mfma_f32_4x4x4_16b_bf16 %4, v[148:149], v[128:129], %4\n" "v_mfma_f32_4x4x4_16b_bf16 %0, v[150:151], v[126:127], %0\n" "v_readlane_b32 s38, %5, 14\n" "v_mfma_f32_4x4x4_16b_bf16 %1, v[152:153], v[126:127], %1\n" "v_perm_b32 v128, s37, s37, %6\n" "v_mfma_f32_4x4x4_16b_bf16 %2, v[154:155], v[126:127], %2\n" "v_perm_b32 v129, s37, s37, %7\n" "v_mfma_f32_4x4x4_16b_bf16 %3, v[156:157], v[126:127], %3\n" "v_mfma_f32_4x4x4_16b_bf16 %4, v[158:159], v[126:127], %4\n" "v_mfma_f32_4x4x4_16b_bf16 %0, v[160:161], v[128:129], %0\n" "v_readlane_b32 s39, %5, 15\n" "v_mfma_f32_4x4x4_16b_bf16 %1, v[162:163], v[128:129], %1\n" "v_perm_b32 v126, s38, s38, %6\n" "v_mfma_f32_4x4x4_16b_bf16 %2, v[164:165], v[128:129], %2\n" "v_perm_b32 v127, s38, s38, %7\n" "v_mfma_f32_4x4x4_16b_bf16 %3, v[166:167], v[128:129], %3\n" "v_mfma_f32_4x4x4_16b_bf16 %4, v[168:169], v[128:129], %4\n" "v_mfma_f32_4x4x4_16b_bf16 %0, v[170:171], v[126:127], %0\n" "v_readlane_b32 s36, %5, 16\n" "v_mfma_f32_4x4x4_16b_bf16 %1, v[172:173], v[126:127], %1\n" "v_perm_b32 v128, s39, s39, %6\n" "v_mfma_f32_4x4x4_16b_bf16 %2, v[174:175], v[126:127], %2\n" "v_perm_b32 v129, s39, s39, %7\n" "v_mfma_f32_4x4x4_16b_bf16 %3, v[176:177], v[126:127], %3\n" "v_mfma_f32_4x4x4_16b_bf16 %4, v[178:179], v[126:127], %4\n" "v_mfma_f32_4x4x4_16b_bf16 %0, v[180:181], v[128:129], %0\n" "v_readlane_b32 s37, %5, 17\n" "v_mfma_f32_4x4x4_16b_bf16 %1, v[182:183], v[128:129], %1\n" "v_perm_b32 v126, s36, s36, %6\n" "v_mfma_f32_4x4x4_16b_bf16 %2, v[184:185], v[128:129], %2\n" "v_perm_b32 v127, s36, s36, %7\n" "v_mfma_f32_4x4x4_16b_bf16 %3, v[186:187], v[128:129], %3\n" "v_mfma_f32_4x4x4_16b_bf16 %4, v[188:189], v[128:129], %4\n" "v_mfma_f32_4x4x4_16b_bf16 %0, v[190:191], v[126:127], %0\n" "v_readlane_b32 s38, %5, 18\n" "v_mfma_f32_4x4x4_16b_bf16 %1, v[192:193], v[126:127], %1\n" "v_perm_b32 v128, s37, s37, %6\n" "v_mfma_f32_4x4x4_16b_bf16 %2, v[194:195], v[126:127], %2\n" "v_perm_b32 v129, s37, s37, %7\n" "v_mfma_f32_4x4x4_16b_bf16 %3, v[196:197], v[126:127], %3\n" "v_mfma_f32_4x4x4_16b_bf16 %4, v[198:199], v[126:127], %4\n" "v_mfma_f32_4x4x4_16b_bf16 %0, v[48:49], v[128:129], %0\n" "v_readlane_b32 s39, %5, 19\n" "v_mfma_f32_4x4x4_16b_bf16 %1, v[50:51], v[128:129], %1\n" "v_perm_b32 v126, s38, s38, %6\n" "v_mfma_f32_4x4x4_16b_bf16 %2, v[52:53], v[128:129], %2\n" "v_perm_b32 v127, s38, s38, %7\n" "v_mfma_f32_4x4x4_16b_bf16 %3, v[54:55], v[128:129], %3\n" "v_mfma_f32_4x4x4_16b_bf16 %4, v[56:57], v[128:129], %4\n" "v_mfma_f32_4x4x4_16b_bf16 %0, v[58:59], v[126:127], %0\n" "v_readlane_b32 s36, %5, 20\n" "v_mfma_f32_4x4x4_16b_bf16 %1, v[60:61], v[126:127], %1\n" "v_perm_b32 v128, s39, s39, %6\n" "v_mfma_f32_4x4x4_16b_bf16 %2, v[62:63], v[126:127], %2\n" "v_perm_b32 v129, s39, s39, %7\n" "v_mfma_f32_4x4x4_16b_bf16 %3, v[64:65], v[126:127], %3\n" "v_mfma_f32_4x4x4_16b_bf16 %4, v[66:67], v[126:127], %4\n" "v_mfma_f32_4x4x4_16b_bf16 %0, v[68:69], v[128:129], %0\n" "v_readlane_b32 s37, %5, 21\n" "v_mfma_f32_4x4x4_16b_bf16 %1, v[70:71], v[128:129], %1\n" "v_perm_b32 v126, s36, s36, %6\n" "v_mfma_f32_4x4x4_16b_bf16 %2, v[72:73], v[128:129], %2\n" "v_perm_b32 v127, s36, s36, %7\n" "v_mfma_f32_4x4x4_16b_bf16 %3, v[74:75], v[128:129], %3\n" "v_mfma_f32_4x4x4_16b_bf16 %4, v[76:77], v[128:129], %4\n" "v_mfma_f32_4x4x4_16b_bf16 %0, v[78:79], v[126:127], %0\n" "v_readlane_b32 s38, %5, 22\n" "v_mfma_f32_4x4x4_16b_bf16 %1, v[80:81], v[126:127], %1\n" "v_perm_b32 v128, s37, s37, %6\n" "v_mfma_f32_4x4x4_16b_bf16 %2, v[82:83], v[126:127], %2\n" "v_perm_b32 v129, s37, s37, %7\n" "v_mfma_f32_4x4x4_16b_bf16 %3, v[84:85], v[126:127], %3\n" "v_mfma_f32_4x4x4_16b_bf16 %4, v[86:87], v[126:127], %4\n" "v_mfma_f32_4x4x4_16b_bf16 %0, v[88:89], v[128:129], %0\n" "v_readlane_b32 s39, %5, 23\n" "v_mfma_f32_4x4x4_16b_bf16 %1, v[90:91], v[128:129], %1\n" "v_perm_b32 v126, s38, s38, %6\n" "v_mfma_f32_4x4x4_16b_bf16 %2, v[92:93], v[128:129], %2\n" "v_perm_b32 v127, s38, s38, %7\n" "v_mfma_f32_4x4x4_16b_bf16 %3, v[94:95], v[128:129], %3\n" "v_mfma_f32_4x4x4_16b_bf16 %4, v[96:97], v[128:129], %4\n" "v_mfma_f32_4x4x4_16b_bf16 %0, v[98:99], v[126:127], %0\n" "v_perm_b32 v128, s39, s39, %6\n" "v_mfma_f32_4x4x4_16b_bf16 %1, v[100:101], v[126:127], %1\n" "v_perm_b32 v129, s39, s39, %7\n" "v_mfma_f32_4x4x4_16b_bf16 %2, v[102:103], v[126:127], %2\n" "v_mfma_f32_4x4x4_16b_bf16 %3, v[104:105], v[126:127], %3\n" "v_mfma_f32_4x4x4_16b_bf16 %4, v[106:107], v[126:127], %4\n" "v_mfma_f32_4x4x4_16b_bf16 %0, v[108:109], v[128:129], %0\n" "v_mfma_f32_4x4x4_16b_bf16 %1, v[110:111], v[128:129], %1\n" "v_mfma_f32_4x4x4_16b_bf16 %2, v[112:113], v[128:129], %2\n" "v_mfma_f32_4x4x4_16b_bf16 %3, v[114:115], v[128:129], %3\n" "v_mfma_f32_4x4x4_16b_bf16 %4, v[116:117], v[128:129], %4\n" "s_nop 7\n" : "=&v"(dd[0]), "=&v"(dd[1]), "=&v"(dd[2]), "=&v"(dd[3]), "=&v"(dd[4]) : "v"(pr), "v"(sel0), "v"(sel1) : GV_CLOB);
    }
    publish640(dd, W_Y, ytag);
    CSTAMP(5 + 6 * n);
    if (ps == 0) GATHER(    "s_mov_b32 s36, 15\n" "s_mov_b32 s37, 0\n" "s_mov_b64 s[40:41], -1\n" "s_mov_b64 s[42:43], -1\n" "s_mov_b64 s[44:45], -1\n" "s_mov_b64 s[46:47], -1\n" "s_barrier\n" "s_cmp_eq_u32 %7, 0\n" "s_cbranch_scc0 800f\n" "s_mov_b32 s40, 0\n" "s_not_b64 exec, s[40:41]\n" "ds_read_b128 v[48:51], %8 offset:0\n" "ds_read_b128 v[52:55], %8 offset:512\n" "ds_read_b128 v[56:59], %8 offset:1024\n" "ds_read_b128 v[60:63], %8 offset:1536\n" "ds_read_b128 v[64:67], %8 offset:2048\n" "s_mov_b64 exec, -1\n" "800:\n" "s_cmp_eq_u32 %7, 4\n" "s_cbranch_scc0 801f\n" "s_mov_b32 s41, 0\n" "s_not_b64 exec, s[40:41]\n" "ds_read_b128 v[48:51], %8 offset:0\n" "ds_read_b128 v[52:55], %8 offset:512\n" "ds_read_b128 v[56:59], %8 offset:1024\n" "ds_read_b128 v[60:63], %8 offset:1536\n" "ds_read_b128 v[64:67], %8 offset:2048\n" "s_mov_b64 exec, -1\n" "801:\n" "s_cmp_eq_u32 %7, 1\n" "s_cbranch_scc0 810f\n" "s_mov_b32 s42, 0\n" "s_not_b64 exec, s[42:43]\n" "ds_read_b128 v[68:71], %8 offset:0\n" "ds_read_b128 v[72:75], %8 offset:512\n" "ds_read_b128 v[76:79], %8 offset:1024\n" "ds_read_b128 v[80:83], %8 offset:1536\n" "ds_read_b128 v[84:87], %8 offset:2048\n" "s_mov_b64 exec, -1\n" "810:\n" "s_cmp_eq_u32 %7, 5\n" "s_cbranch_scc0 811f\n" "s_mov_b32 s43, 0\n" "s_not_b64 exec, s[42:43]\n" "ds_read_b128 v[68:71], %8 offset:0\n" "ds_read_b128 v[72:75], %8 offset:512\n" "ds_read_b128 v[76:79], %8 offset:1024\n" "ds_read_b128 v[80:83], %8 offset:1536\n" "ds_read_b128 v[84:87], %8 offset:2048\n" "s_mov_b64 exec, -1\n" "811:\n" "s_cmp_eq_u32 %7, 2\n" "s_cbranch_scc0 820f\n" "s_mov_b32 s44, 0\n" "s_not_b64 exec, s[44:45]\n" "ds_read_b128 v[88:91], %8 offset:0\n" "ds_read_b128 v[92:95], %8 offset:512\n" "ds_read_b128 v[96:99], %8 offset:1024\n" "ds_read_b128 v[100:103], %8 offset:1536\n" "ds_read_b128 v[104:107], %8 offset:2048\n" "s_mov_b64 exec, -1\n" "820:\n" "s_cmp_eq_u32 %7, 6\n" "s_cbranch_scc0 821f\n" "s_mov_b32 s45, 0\n" "s_not_b64 exec, s[44:45]\n" "ds_read_b128 v[88:91], %8 offset:0\n" "ds_read_b128 v[92:95], %8 offset:512\n" "ds_read_b128 v[96:99], %8 offset:1024\n" "ds_read_b128 v[100:103], %8 offset:1536\n" "ds_read_b128 v[104:107], %8 offset:2048\n" "s_mov_b64 exec, -1\n" "821:\n" "s_cmp_eq_u32 %7, 3\n" "s_cbranch_scc0 830f\n" "s_mov_b32 s46, 0\n" "s_not_b64 exec, s[46:47]\n" "ds_read_b128 v[108:111], %8 offset:0\n" "ds_read_b128 v[112:115], %8 offset:512\n" "ds_read_b128 v[116:119], %8 offset:1024\n" "ds_read_b128 v[120:123], %8 offset:1536\n" "ds_read_b128 v[124:127], %8 offset:2048\n" "s_mov_b64 exec, -1\n" "830:\n" "s_cmp_eq_u32 %7, 7\n" "s_cbranch_scc0 831f\n" "s_mov_b32 s47, 0\n" "s_not_b64 exec, s[46:47]\n" "ds_read_b128 v[108:111], %8 offset:0\n" "ds_read_b128 v[112:115], %8 offset:512\n" "ds_read_b128 v[116:119], %8 offset:1024\n" "ds_read_b128 v[120:123], %8 offset:1536\n" "ds_read_b128 v[124:127], %8 offset:2048\n" "s_mov_b64 exec, -1\n" "831:\n" "s_movk_i32 s38, 0\n" "s_mov_b64 exec, s[40:41]\n" "buffer_load_dwordx4 v[48:51], %0, %1, s38 offen offset:0 sc1\n" "buffer_load_dwordx4 v[52:55], %0, %1, s38 offen offset:512 sc1\n" "buffer_load_dwordx4 v[56:59], %0, %1, s38 offen offset:1024 sc1\n" "buffer_load_dwordx4 v[60:63], %0, %1, s38 offen offset:1536 sc1\n" "buffer_load_dwordx4 v[64:67], %0, %1, s38 offen offset:2048 sc1\n" "s_movk_i32 s38, 2560\n" "s_mov_b64 exec, s[42:43]\n" "buffer_load_dwordx4 v[68:71], %0, %1, s38 offen offset:0 sc1\n" "buffer_load_dwordx4 v[72:75], %0, %1, s38 offen offset:512 sc1\n" "buffer_load_dwordx4 v[76:79], %0, %1, s38 offen offset:1024 sc1\n" "buffer_load_dwordx4 v[80:83], %0, %1, s38 offen offset:1536 sc1\n" "buffer_load_dwordx4 v[84:87], %0, %1, s38 offen offset:2048 sc1\n" "s_movk_i32 s38, 5120\n" "s_mov_b64 exec, s[44:45]\n" "buffer_load_dwordx4 v[88:91], %0, %1, s38 offen offset:0 sc1\n" "buffer_load_dwordx4 v[92:95], %0, %1, s38 offen offset:512 sc1\n" "buffer_load_dwordx4 v[96:99], %0, %1, s38 offen offset:1024 sc1\n" "buffer_load_dwordx4 v[100:103], %0, %1, s38 offen offset:1536 sc1\n" "buffer_load_dwordx4 v[104:107], %0, %1, s38 offen offset:2048 sc1\n" "s_movk_i32 s38, 7680\n" "s_mov_b64 exec, s[46:47]\n" "buffer_load_dwordx4 v[108:111], %0, %1, s38 offen offset:0 sc1\n" "buffer_load_dwordx4 v[112:115], %0, %1, s38 offen offset:512 sc1\n" "buffer_load_dwordx4 v[116:119], %0, %1, s38 offen offset:1024 sc1\n" "buffer_load_dwordx4 v[120:123], %0, %1, s38 offen offset:1536 sc1\n" "buffer_load_dwordx4 v[124:127], %0, %1, s38 offen offset:2048 sc1\n" "s_mov_b64 exec, -1\n" "s_mov_b32 s39, 700416\n" "buffer_load_dword v130, %4, %5, s39 offen offset:0\n" "buffer_load_dword v132, %4, %5, s39 offen offset:128\n" "buffer_load_dword v134, %4, %5, s39 offen offset:256\n" "s_mov_b32 s39, 724992\n" "buffer_load_dword v131, %4, %5, s39 offen offset:0\n" "buffer_load_dword v133, %4, %5, s39 offen offset:128\n" "buffer_load_dword v135, %4, %5, s39 offen offset:256\n" "s_mov_b32 s39, 737280\n" "buffer_load_dword v136, %4, %5, s39 offen offset:0\n" "buffer_load_dword v138, %4, %5, s39 offen offset:128\n" "buffer_load_dword v140, %4, %5, s39 offen offset:256\n" "s_mov_b32 s39, 761856\n" "buffer_load_dword v137, %4, %5, s39 offen offset:0\n" "buffer_load_dword v139, %4, %5, s39 offen offset:128\n" "buffer_load_dword v141, %4, %5, s39 offen offset:256\n" "s_mov_b32 s39, 749568\n" "buffer_load_dword v142, %4, %5, s39 offen offset:0\n" "buffer_load_dword v144, %4, %5, s39 offen offset:128\n" "buffer_load_dword v146, %4, %5, s39 offen offset:256\n" "s_mov_b32 s39, 774144\n" "buffer_load_dword v143, %4, %5, s39 offen offset:0\n" "buffer_load_dword v145, %4, %5, s39 offen offset:128\n" "buffer_load_dword v147, %4, %5, s39 offen offset:256\n" "s_mov_b32 s39, 786432\n" "buffer_load_dword v148, %4, %5, s39 offen offset:0\n" "buffer_load_dword v150, %4, %5, s39 offen offset:128\n" "buffer_load_dword v152, %4, %5, s39 offen offset:256\n" "s_mov_b32 s39, 811008\n" "buffer_load_dword v149, %4, %5, s39 offen offset:0\n" "buffer_load_dword v151, %4, %5, s39 offen offset:128\n" "buffer_load_dword v153, %4, %5, s39 offen offset:256\n" "s_mov_b32 s39, 798720\n" "buffer_load_dword v154, %4, %5, s39 offen offset:0\n" "buffer_load_dword v156, %4, %5, s39 offen offset:128\n" "buffer_load_dword v158, %4, %5, s39 offen offset:256\n" "s_mov_b32 s39, 823296\n" "buffer_load_dword v155, %4, %5, s39 offen offset:0\n" "buffer_load_dword v157, %4, %5, s39 offen offset:128\n" "buffer_load_dword v159, %4, %5, s39 offen offset:256\n" "s_mov_b32 s39, 835584\n" "buffer_load_dword v160, %4, %5, s39 offen offset:0\n" "buffer_load_dword v162, %4, %5, s39 offen offset:128\n" "buffer_load_dword v164, %4, %5, s39 offen offset:256\n" "s_mov_b32 s39, 860160\n" "buffer_load_dword v161, %4, %5, s39 offen offset:0\n" "buffer_load_dword v163, %4, %5, s39 offen offset:128\n" "buffer_load_dword v165, %4, %5, s39 offen offset:256\n" "s_mov_b32 s39, 847872\n" "buffer_load_dword v166, %4, %5, s39 offen offset:0\n" "buffer_load_dword v168, %4, %5, s39 offen offset:128\n" "buffer_load_dword v170, %4, %5, s39 offen offset:256\n" "s_mov_b32 s39, 872448\n" "buffer_load_dword v167, %4, %5, s39 offen offset:0\n" "buffer_load_dword v169, %4, %5, s39 offen offset:128\n" "buffer_load_dword v171, %4, %5, s39 offen offset:256\n" "s_waitcnt vmcnt(42)\n" "s_mov_b32 s39, 884736\n" "buffer_load_dword v172, %4, %5, s39 offen offset:0\n" "buffer_load_dword v174, %4, %5, s39 offen offset:128\n" "buffer_load_dword v176, %4, %5, s39 offen offset:256\n" "s_mov_b32 s39, 909312\n" "buffer_load_dword v173, %4, %5, s39 offen offset:0\n" "buffer_load_dword v175, %4, %5, s39 offen offset:128\n" "buffer_load_dword v177, %4, %5, s39 offen offset:256\n" "s_mov_b32 s39, 897024\n" "buffer_load_dword v178, %4, %5, s39 offen offset:0\n" "buffer_load_dword v180, %4, %5, s39 offen offset:128\n" "buffer_load_dword v182, %4, %5, s39 offen offset:256\n" "s_mov_b32 s39, 921600\n" "buffer_load_dword v179, %4, %5, s39 offen offset:0\n" "buffer_load_dword v181, %4, %5, s39 offen offset:128\n" "buffer_load_dword v183, %4, %5, s39 offen offset:256\n" "s_mov_b32 s39, 933888\n" "buffer_load_dword v184, %4, %5, s39 offen offset:0\n" "buffer_load_dword v186, %4, %5, s39 offen offset:128\n" "buffer_load_dword v188, %4, %5, s39 offen offset:256\n" "s_mov_b32 s39, 958464\n" "buffer_load_dword v185, %4, %5, s39 offen offset:0\n" "buffer_load_dword v187, %4, %5, s39 offen offset:128\n" "buffer_load_dword v189, %4, %5, s39 offen offset:256\n" "s_mov_b32 s39, 946176\n" "buffer_load_dword v190, %4, %5, s39 offen offset:0\n" "buffer_load_dword v192, %4, %5, s39 offen offset:128\n" "buffer_load_dword v194, %4, %5, s39 offen offset:256\n" "s_mov_b32 s39, 970752\n" "buffer_load_dword v191, %4, %5, s39 offen offset:0\n" "buffer_load_dword v193, %4, %5, s39 offen offset:128\n" "buffer_load_dword v195, %4, %5, s39 offen offset:256\n" "s_branch 5f\n" "1:\n" "s_bitcmp1_b32 s36, 0\n" "s_cbranch_scc0 20f\n" "s_movk_i32 s38, 0\n" "s_mov_b64 exec, s[40:41]\n" "buffer_load_dwordx4 v[48:51], %0, %1, s38 offen offset:0 sc1\n" "buffer_load_dwordx4 v[52:55], %0, %1, s38 offen offset:512 sc1\n" "buffer_load_dwordx4 v[56:59], %0, %1, s38 offen offset:1024 sc1\n" "buffer_load_dwordx4 v[60:63], %0, %1, s38 offen offset:1536 sc1\n" "buffer_load_dwordx4 v[64:67], %0, %1, s38 offen offset:2048 sc1\n" "20:\n" "s_bitcmp1_b32 s36, 1\n" "s_cbranch_scc0 21f\n" "s_movk_i32 s38, 2560\n" "s_mov_b64 exec, s[42:43]\n" "buffer_load_dwordx4 v[68:71], %0, %1, s38 offen offset:0 sc1\n" "buffer_load_dwordx4 v[72:75], %0, %1, s38 offen offset:512 sc1\n" "buffer_load_dwordx4 v[76:79], %0, %1, s38 offen offset:1024 sc1\n" "buffer_load_dwordx4 v[80:83], %0, %1, s38 offen offset:1536 sc1\n" "buffer_load_dwordx4 v[84:87], %0, %1, s38 offen offset:2048 sc1\n" "21:\n" "s_bitcmp1_b32 s36, 2\n" "s_cbranch_scc0 22f\n" "s_movk_i32 s38, 5120\n" "s_mov_b64 exec, s[44:45]\n" "buffer_load_dwordx4 v[88:91], %0, %1, s38 offen offset:0 sc1\n" "buffer_load_dwordx4 v[92:95], %0, %1, s38 offen offset:512 sc1\n" "buffer_load_dwordx4 v[96:99], %0, %1, s38 offen offset:1024 sc1\n" "buffer_load_dwordx4 v[100:103], %0, %1, s38 offen offset:1536 sc1\n" "buffer_load_dwordx4 v[104:107], %0, %1, s38 offen offset:2048 sc1\n" "22:\n" "s_bitcmp1_b32 s36, 3\n" "s_cbranch_scc0 23f\n" "s_movk_i32 s38, 7680\n" "s_mov_b64 exec, s[46:47]\n" "buffer_load_dwordx4 v[108:111], %0, %1, s38 offen offset:0 sc1\n" "buffer_load_dwordx4 v[112:115], %0, %1, s38 offen offset:512 sc1\n" "buffer_load_dwordx4 v[116:119], %0, %1, s38 offen offset:1024 sc1\n" "buffer_load_dwordx4 v[120:123], %0, %1, s38 offen offset:1536 sc1\n" "buffer_load_dwordx4 v[124:127], %0, %1, s38 offen offset:2048 sc1\n" "23:\n" "s_mov_b64 exec, -1\n" "s_waitcnt vmcnt(0)\n" "5:\n" "s_waitcnt lgkmcnt(0)\n" "s_bitcmp1_b32 s36, 0\n" "s_cbranch_scc0 30f\n" "v_bitop3_b32 v128, v48, v49, v50 bitop3:0x80\n" "v_bitop3_b32 v129, v51, v52, v53 bitop3:0x80\n" "v_bitop3_b32 v128, v128, v54, v55 bitop3:0x80\n" "v_bitop3_b32 v129, v129, v56, v57 bitop3:0x80\n" "v_bitop3_b32 v128, v128, v58, v59 bitop3:0x80\n" "v_bitop3_b32 v129, v129, v60, v61 bitop3:0x80\n" "v_bitop3_b32 v128, v128, v62, v63 bitop3:0x80\n" "v_bitop3_b32 v129, v129, v64, v65 bitop3:0x80\n" "v_bitop3_b32 v128, v128, v66, v67 bitop3:0x80\n" "v_bitop3_b32 v128, v128, v129, %2 bitop3:0x80\n" "v_cmp_eq_u32 vcc, 0, v128\n" "s_or_b64 s[40:41], vcc, vcc\n" "s_cbranch_scc1 30f\n" "s_bitset0_b32 s36, 0\n" "30:\n" "s_bitcmp1_b32 s36, 1\n" "s_cbranch_scc0 31f\n" "v_bitop3_b32 v128, v68, v69, v70 bitop3:0x80\n" "v_bitop3_b32 v129, v71, v72, v73 bitop3:0x80\n" "v_bitop3_b32 v128, v128, v74, v75 bitop3:0x80\n" "v_bitop3_b32 v129, v129, v76, v77 bitop3:0x80\n" "v_bitop3_b32 v128, v128, v78, v79 bitop3:0x80\n" "v_bitop3_b32 v129, v129, v80, v81 bitop3:0x80\n" "v_bitop3_b32 v128, v128, v82, v83 bitop3:0x80\n" "v_bitop3_b32 v129, v129, v84, v85 bitop3:0x80\n" "v_bitop3_b32 v128, v128, v86, v87 bitop3:0x80\n" "v_bitop3_b32 v128, v128, v129, %2 bitop3:0x80\n" "v_cmp_eq_u32 vcc, 0, v128\n" "s_or_b64 s[42:43], vcc, vcc\n" "s_cbranch_scc1 31f\n" "s_bitset0_b32 s36, 1\n" "31:\n" "s_bitcmp1_b32 s36, 2\n" "s_cbranch_scc0 32f\n" "v_bitop3_b32 v128, v88, v89, v90 bitop3:0x80\n" "v_bitop3_b32 v129, v91, v92, v93 bitop3:0x80\n" "v_bitop3_b32 v128, v128, v94, v95 bitop3:0x80\n" "v_bitop3_b32 v129, v129, v96, v97 bitop3:0x80\n" "v_bitop3_b32 v128, v128, v98, v99 bitop3:0x80\n" "v_bitop3_b32 v129, v129, v100, v101 bitop3:0x80\n" "v_bitop3_b32 v128, v128, v102, v103 bitop3:0x80\n" "v_bitop3_b32 v129, v129, v104, v105 bitop3:0x80\n" "v_bitop3_b32 v128, v128, v106, v107 bitop3:0x80\n" "v_bitop3_b32 v128, v128, v129, %2 bitop3:0x80\n" "v_cmp_eq_u32 vcc, 0, v128\n" "s_or_b64 s[44:45], vcc, vcc\n" "s_cbranch_scc1 32f\n" "s_bitset0_b32 s36, 2\n" "32:\n" "s_bitcmp1_b32 s36, 3\n" "s_cbranch_scc0 33f\n" "v_bitop3_b32 v128, v108, v109, v110 bitop3:0x80\n" "v_bitop3_b32 v129, v111, v112, v113 bitop3:0x80\n" "v_bitop3_b32 v128, v128, v114, v115 bitop3:0x80\n" "v_bitop3_b32 v129, v129, v116, v117 bitop3:0x80\n" "v_bitop3_b32 v128, v128, v118, v119 bitop3:0x80\n" "v_bitop3_b32 v129, v129, v120, v121 bitop3:0x80\n" "v_bitop3_b32 v128, v128, v122, v123 bitop3:0x80\n" "v_bitop3_b32 v129, v129, v124, v125 bitop3:0x80\n" "v_bitop3_b32 v128, v128, v126, v127 bitop3:0x80\n" "v_bitop3_b32 v128, v128, v129, %2 bitop3:0x80\n" "v_cmp_eq_u32 vcc, 0, v128\n" "s_or_b64 s[46:47], vcc, vcc\n" "s_cbranch_scc1 33f\n" "s_bitset0_b32 s36, 3\n" "33:\n" "s_cmp_eq_u32 s36, 0\n" "s_cbranch_scc1 4f\n" "6:\n" "s_add_u32 s37, s37, 1\n" "s_cmp_lt_u32 s37, 131072\n" "s_cbranch_scc0 7f\n" "s_branch 1b\n" "buffer_load_dword v128, %6, %1, 0 offen sc1\n" "s_waitcnt vmcnt(0)\n" "v_and_b32 v128, %2, v128\n" "v_cmp_ne_u32 vcc, 0, v128\n" "s_cmp_eq_u64 vcc, -1\n" "s_cbranch_scc1 1b\n" "s_branch 6b\n" "7:\n" "v_mov_b32 v128, 0x3e8\n" "v_mov_b32 v129, 0\n" "buffer_store_dword v128, v129, %1, 0 offen sc1\n" "4:\n" "v_pk_add_f32 v[48:49], v[48:49], v[68:69]\n" "v_pk_add_f32 v[88:89], v[88:89], v[108:109]\n" "v_pk_add_f32 v[50:51], v[50:51], v[70:71]\n" "v_pk_add_f32 v[90:91], v[90:91], v[110:111]\n" "v_pk_add_f32 v[52:53], v[52:53], v[72:73]\n" "v_pk_add_f32 v[92:93], v[92:93], v[112:113]\n" "v_pk_add_f32 v[54:55], v[54:55], v[74:75]\n" "v_pk_add_f32 v[94:95], v[94:95], v[114:115]\n" "v_pk_add_f32 v[56:57], v[56:57], v[76:77]\n" "v_pk_add_f32 v[96:97], v[96:97], v[116:117]\n" "v_pk_add_f32 v[58:59], v[58:59], v[78:79]\n" "v_pk_add_f32 v[98:99], v[98:99], v[118:119]\n" "v_pk_add_f32 v[60:61], v[60:61], v[80:81]\n" "v_pk_add_f32 v[100:101], v[100:101], v[120:121]\n" "v_pk_add_f32 v[62:63], v[62:63], v[82:83]\n" "v_pk_add_f32 v[102:103], v[102:103], v[122:123]\n" "v_pk_add_f32 v[64:65], v[64:65], v[84:85]\n" "v_pk_add_f32 v[104:105], v[104:105], v[124:125]\n" "v_pk_add_f32 v[66:67], v[66:67], v[86:87]\n" "v_pk_add_f32 v[106:107], v[106:107], v[126:127]\n" "v_pk_add_f32 v[48:49], v[48:49], v[88:89]\n" "v_pk_add_f32 v[50:51], v[50:51], v[90:91]\n" "v_pk_add_f32 v[52:53], v[52:53], v[92:93]\n" "v_pk_add_f32 v[54:55], v[54:55], v[94:95]\n" "v_pk_add_f32 v[56:57], v[56:57], v[96:97]\n" "v_pk_add_f32 v[58:59], v[58:59], v[98:99]\n" "v_pk_add_f32 v[60:61], v[60:61], v[100:101]\n" "v_pk_add_f32 v[62:63], v[62:63], v[102:103]\n" "v_pk_add_f32 v[64:65], v[64:65], v[104:105]\n" "v_pk_add_f32 v[66:67], v[66:67], v[106:107]\n" "s_nop 1\n" "v_permlane32_swap_b32 v48, v50\n" "v_permlane32_swap_b32 v49, v51\n" "v_permlane32_swap_b32 v52, v54\n" "v_permlane32_swap_b32 v53, v55\n" "v_permlane32_swap_b32 v56, v58\n" "v_permlane32_swap_b32 v57, v59\n" "v_permlane32_swap_b32 v60, v62\n" "v_permlane32_swap_b32 v61, v63\n" "v_permlane32_swap_b32 v64, v66\n" "v_permlane32_swap_b32 v65, v67\n" "s_nop 1\n" "v_pk_add_f32 v[48:49], v[48:49], v[50:51]\n" "v_pk_add_f32 v[52:53], v[52:53], v[54:55]\n" "v_pk_add_f32 v[56:57], v[56:57], v[58:59]\n" "v_pk_add_f32 v[60:61], v[60:61], v[62:63]\n" "v_pk_add_f32 v[64:65], v[64:65], v[66:67]\n" "s_barrier\n" "ds_write_b64 %3, v[48:49] offset:0\n" "ds_write_b64 %3, v[52:53] offset:512\n" "ds_write_b64 %3, v[56:57] offset:1024\n" "ds_write_b64 %3, v[60:61] offset:1536\n" "ds_write_b64 %3, v[64:65] offset:2048\n" "s_waitcnt lgkmcnt(0)\n" "s_barrier\n", W_Y, ytag, wq_off, rq); else GATHER(    "s_mov_b32 s36, 15\n" "s_mov_b32 s37, 0\n" "s_mov_b64 s[40:41], -1\n" "s_mov_b64 s[42:43], -1\n" "s_mov_b64 s[44:45], -1\n" "s_mov_b64 s[46:47], -1\n" "s_barrier\n" "s_cmp_eq_u32 %7, 0\n" "s_cbranch_scc0 800f\n" "s_mov_b32 s40, 0\n" "s_not_b64 exec, s[40:41]\n" "ds_read_b128 v[48:51], %8 offset:0\n" "ds_read_b128 v[52:55], %8 offset:512\n" "ds_read_b128 v[56:59], %8 offset:1024\n" "ds_read_b128 v[60:63], %8 offset:1536\n" "ds_read_b128 v[64:67], %8 offset:2048\n" "s_mov_b64 exec, -1\n" "800:\n" "s_cmp_eq_u32 %7, 4\n" "s_cbranch_scc0 801f\n" "s_mov_b32 s41, 0\n" "s_not_b64 exec, s[40:41]\n" "ds_read_b128 v[48:51], %8 offset:0\n" "ds_read_b128 v[52:55], %8 offset:512\n" "ds_read_b128 v[56:59], %8 offset:1024\n" "ds_read_b128 v[60:63], %8 offset:1536\n" "ds_read_b128 v[64:67], %8 offset:2048\n" "s_mov_b64 exec, -1\n" "801:\n" "s_cmp_eq_u32 %7, 1\n" "s_cbranch_scc0 810f\n" "s_mov_b32 s42, 0\n" "s_not_b64 exec, s[42:43]\n" "ds_read_b128 v[68:71], %8 offset:0\n" "ds_read_b128 v[72:75], %8 offset:512\n" "ds_read_b128 v[76:79], %8 offset:1024\n" "ds_read_b128 v[80:83], %8 offset:1536\n" "ds_read_b128 v[84:87], %8 offset:2048\n" "s_mov_b64 exec, -1\n" "810:\n" "s_cmp_eq_u32 %7, 5\n" "s_cbranch_scc0 811f\n" "s_mov_b32 s43, 0\n" "s_not_b64 exec, s[42:43]\n" "ds_read_b128 v[68:71], %8 offset:0\n" "ds_read_b128 v[72:75], %8 offset:512\n" "ds_read_b128 v[76:79], %8 offset:1024\n" "ds_read_b128 v[80:83], %8 offset:1536\n" "ds_read_b128 v[84:87], %8 offset:2048\n" "s_mov_b64 exec, -1\n" "811:\n" "s_cmp_eq_u32 %7, 2\n" "s_cbranch_scc0 820f\n" "s_mov_b32 s44, 0\n" "s_not_b64 exec, s[44:45]\n" "ds_read_b128 v[88:91], %8 offset:0\n" "ds_read_b128 v[92:95], %8 offset:512\n" "ds_read_b128 v[96:99], %8 offset:1024\n" "ds_read_b128 v[100:103], %8 offset:1536\n" "ds_read_b128 v[104:107], %8 offset:2048\n" "s_mov_b64 exec, -1\n" "820:\n" "s_cmp_eq_u32 %7, 6\n" "s_cbranch_scc0 821f\n" "s_mov_b32 s45, 0\n" "s_not_b64 exec, s[44:45]\n" "ds_read_b128 v[88:91], %8 offset:0\n" "ds_read_b128 v[92:95], %8 offset:512\n" "ds_read_b128 v[96:99], %8 offset:1024\n" "ds_read_b128 v[100:103], %8 offset:1536\n" "ds_read_b128 v[104:107], %8 offset:2048\n" "s_mov_b64 exec, -1\n" "821:\n" "s_cmp_eq_u32 %7, 3\n" "s_cbranch_scc0 830f\n" "s_mov_b32 s46, 0\n" "s_not_b64 exec, s[46:47]\n" "ds_read_b128 v[108:111], %8 offset:0\n" "ds_read_b128 v[112:115], %8 offset:512\n" "ds_read_b128 v[116:119], %8 offset:1024\n" "ds_read_b128 v[120:123], %8 offset:1536\n" "ds_read_b128 v[124:127], %8 offset:2048\n" "s_mov_b64 exec, -1\n" "830:\n" "s_cmp_eq_u32 %7, 7\n" "s_cbranch_scc0 831f\n" "s_mov_b32 s47, 0\n" "s_not_b64 exec, s[46:47]\n" "ds_read_b128 v[108:111], %8 offset:0\n" "ds_read_b128 v[112:115], %8 offset:512\n" "ds_read_b128 v[116:119], %8 offset:1024\n" "ds_read_b128 v[120:123], %8 offset:1536\n" "ds_read_b128 v[124:127], %8 offset:2048\n" "s_mov_b64 exec, -1\n" "831:\n" "1:\n" "s_bitcmp1_b32 s36, 0\n" "s_cbranch_scc0 20f\n" "s_movk_i32 s38, 0\n" "s_mov_b64 exec, s[40:41]\n" "buffer_load_dwordx4 v[48:51], %0, %1, s38 offen offset:0 sc1\n" "buffer_load_dwordx4 v[52:55], %0, %1, s38 offen offset:512 sc1\n" "buffer_load_dwordx4 v[56:59], %0, %1, s38 offen offset:1024 sc1\n" "buffer_load_dwordx4 v[60:63], %0, %1, s38 offen offset:1536 sc1\n" "buffer_load_dwordx4 v[64:67], %0, %1, s38 offen offset:2048 sc1\n" "20:\n" "s_bitcmp1_b32 s36, 1\n" "s_cbranch_scc0 21f\n" "s_movk_i32 s38, 2560\n" "s_mov_b64 exec, s[42:43]\n" "buffer_load_dwordx4 v[68:71], %0, %1, s38 offen offset:0 sc1\n" "buffer_load_dwordx4 v[72:75], %0, %1, s38 offen offset:512 sc1\n" "buffer_load_dwordx4 v[76:79], %0, %1, s38 offen offset:1024 sc1\n" "buffer_load_dwordx4 v[80:83], %0, %1, s38 offen offset:1536 sc1\n" "buffer_load_dwordx4 v[84:87], %0, %1, s38 offen offset:2048 sc1\n" "21:\n" "s_bitcmp1_b32 s36, 2\n" "s_cbranch_scc0 22f\n" "s_movk_i32 s38, 5120\n" "s_mov_b64 exec, s[44:45]\n" "buffer_load_dwordx4 v[88:91], %0, %1, s38 offen offset:0 sc1\n" "buffer_load_dwordx4 v[92:95], %0, %1, s38 offen offset:512 sc1\n" "buffer_load_dwordx4 v[96:99], %0, %1, s38 offen offset:1024 sc1\n" "buffer_load_dwordx4 v[100:103], %0, %1, s38 offen offset:1536 sc1\n" "buffer_load_dwordx4 v[104:107], %0, %1, s38 offen offset:2048 sc1\n" "22:\n" "s_bitcmp1_b32 s36, 3\n" "s_cbranch_scc0 23f\n" "s_movk_i32 s38, 7680\n" "s_mov_b64 exec, s[46:47]\n" "buffer_load_dwordx4 v[108:111], %0, %1, s38 offen offset:0 sc1\n" "buffer_load_dwordx4 v[112:115], %0, %1, s38 offen offset:512 sc1\n" "buffer_load_dwordx4 v[116:119], %0, %1, s38 offen offset:1024 sc1\n" "buffer_load_dwordx4 v[120:123], %0, %1, s38 offen offset:1536 sc1\n" "buffer_load_dwordx4 v[124:127], %0, %1, s38 offen offset:2048 sc1\n" "23:\n" "s_mov_b64 exec, -1\n" "s_waitcnt vmcnt(0)\n" "5:\n" "s_waitcnt lgkmcnt(0)\n" "s_bitcmp1_b32 s36, 0\n" "s_cbranch_scc0 30f\n" "v_bitop3_b32 v128, v48, v49, v50 bitop3:0x80\n" "v_bitop3_b32 v129, v51, v52, v53 bitop3:0x80\n" "v_bitop3_b32 v128, v128, v54, v55 bitop3:0x80\n" "v_bitop3_b32 v129, v129, v56, v57 bitop3:0x80\n" "v_bitop3_b32 v128, v128, v58, v59 bitop3:0x80\n" "v_bitop3_b32 v129, v129, v60, v61 bitop3:0x80\n" "v_bitop3_b32 v128, v128, v62, v63 bitop3:0x80\n" "v_bitop3_b32 v129, v129, v64, v65 bitop3:0x80\n" "v_bitop3_b32 v128, v128, v66, v67 bitop3:0x80\n" "v_bitop3_b32 v128, v128, v129, %2 bitop3:0x80\n" "v_cmp_eq_u32 vcc, 0, v128\n" "s_or_b64 s[40:41], vcc, vcc\n" "s_cbranch_scc1 30f\n" "s_bitset0_b32 s36, 0\n" "30:\n" "s_bitcmp1_b32 s36, 1\n" "s_cbranch_scc0 31f\n" "v_bitop3_b32 v128, v68, v69, v70 bitop3:0x80\n" "v_bitop3_b32 v129, v71, v72, v73 bitop3:0x80\n" "v_bitop3_b32 v128, v128, v74, v75 bitop3:0x80\n" "v_bitop3_b32 v129, v129, v76, v77 bitop3:0x80\n" "v_bitop3_b32 v128, v128, v78, v79 bitop3:0x80\n" "v_bitop3_b32 v129, v129, v80, v81 bitop3:0x80\n" "v_bitop3_b32 v128, v128, v82, v83 bitop3:0x80\n" "v_bitop3_b32 v129, v129, v84, v85 bitop3:0x80\n" "v_bitop3_b32 v128, v128, v86, v87 bitop3:0x80\n" "v_bitop3_b32 v128, v128, v129, %2 bitop3:0x80\n" "v_cmp_eq_u32 vcc, 0, v128\n" "s_or_b64 s[42:43], vcc, vcc\n" "s_cbranch_scc1 31f\n" "s_bitset0_b32 s36, 1\n" "31:\n" "s_bitcmp1_b32 s36, 2\n" "s_cbranch_scc0 32f\n" "v_bitop3_b32 v128, v88, v89, v90 bitop3:0x80\n" "v_bitop3_b32 v129, v91, v92, v93 bitop3:0x80\n" "v_bitop3_b32 v128, v128, v94, v95 bitop3:0x80\n" "v_bitop3_b32 v129, v129, v96, v97 bitop3:0x80\n" "v_bitop3_b32 v128, v128, v98, v99 bitop3:0x80\n" "v_bitop3_b32 v129, v129, v100, v101 bitop3:0x80\n" "v_bitop3_b32 v128, v128, v102, v103 bitop3:0x80\n" "v_bitop3_b32 v129, v129, v104, v105 bitop3:0x80\n" "v_bitop3_b32 v128, v128, v106, v107 bitop3:0x80\n" "v_bitop3_b32 v128, v128, v129, %2 bitop3:0x80\n" "v_cmp_eq_u32 vcc, 0, v128\n" "s_or_b64 s[44:45], vcc, vcc\n" "s_cbranch_scc1 32f\n" "s_bitset0_b32 s36, 2\n" "32:\n" "s_bitcmp1_b32 s36, 3\n" "s_cbranch_scc0 33f\n" "v_bitop3_b32 v128, v108, v109, v110 bitop3:0x80\n" "v_bitop3_b32 v129, v111, v112, v113 bitop3:0x80\n" "v_bitop3_b32 v128, v128, v114, v115 bitop3:0x80\n" "v_bitop3_b32 v129, v129, v116, v117 bitop3:0x80\n" "v_bitop3_b32 v128, v128, v118, v119 bitop3:0x80\n" "v_bitop3_b32 v129, v129, v120, v121 bitop3:0x80\n" "v_bitop3_b32 v128, v128, v122, v123 bitop3:0x80\n" "v_bitop3_b32 v129, v129, v124, v125 bitop3:0x80\n" "v_bitop3_b32 v128, v128, v126, v127 bitop3:0x80\n" "v_bitop3_b32 v128, v128, v129, %2 bitop3:0x80\n" "v_cmp_eq_u32 vcc, 0, v128\n" "s_or_b64 s[46:47], vcc, vcc\n" "s_cbranch_scc1 33f\n" "s_bitset0_b32 s36, 3\n" "33:\n" "s_cmp_eq_u32 s36, 0\n" "s_cbranch_scc1 4f\n" "6:\n" "s_add_u32 s37, s37, 1\n" "s_cmp_lt_u32 s37, 131072\n" "s_cbranch_scc0 7f\n" "s_branch 1b\n" "buffer_load_dword v128, %6, %1, 0 offen sc1\n" "s_waitcnt vmcnt(0)\n" "v_and_b32 v128, %2, v128\n" "v_cmp_ne_u32 vcc, 0, v128\n" "s_cmp_eq_u64 vcc, -1\n" "s_cbranch_scc1 1b\n" "s_branch 6b\n" "7:\n" "v_mov_b32 v128, 0x3e8\n" "v_mov_b32 v129, 0\n" "buffer_store_dword v128, v129, %1, 0 offen sc1\n" "4:\n" "v_pk_add_f32 v[48:49], v[48:49], v[68:69]\n" "v_pk_add_f32 v[88:89], v[88:89], v[108:109]\n" "v_pk_add_f32 v[50:51], v[50:51], v[70:71]\n" "v_pk_add_f32 v[90:91], v[90:91], v[110:111]\n" "v_pk_add_f32 v[52:53], v[52:53], v[72:73]\n" "v_pk_add_f32 v[92:93], v[92:93], v[112:113]\n" "v_pk_add_f32 v[54:55], v[54:55], v[74:75]\n" "v_pk_add_f32 v[94:95], v[94:95], v[114:115]\n" "v_pk_add_f32 v[56:57], v[56:57], v[76:77]\n" "v_pk_add_f32 v[96:97], v[96:97], v[116:117]\n" "v_pk_add_f32 v[58:59], v[58:59], v[78:79]\n" "v_pk_add_f32 v[98:99], v[98:99], v[118:119]\n" "v_pk_add_f32 v[60:61], v[60:61], v[80:81]\n" "v_pk_add_f32 v[100:101], v[100:101], v[120:121]\n" "v_pk_add_f32 v[62:63], v[62:63], v[82:83]\n" "v_pk_add_f32 v[102:103], v[102:103], v[122:123]\n" "v_pk_add_f32 v[64:65], v[64:65], v[84:85]\n" "v_pk_add_f32 v[104:105], v[104:105], v[124:125]\n" "v_pk_add_f32 v[66:67], v[66:67], v[86:87]\n" "v_pk_add_f32 v[106:107], v[106:107], v[126:127]\n" "v_pk_add_f32 v[48:49], v[48:49], v[88:89]\n" "v_pk_add_f32 v[50:51], v[50:51], v[90:91]\n" "v_pk_add_f32 v[52:53], v[52:53], v[92:93]\n" "v_pk_add_f32 v[54:55], v[54:55], v[94:95]\n" "v_pk_add_f32 v[56:57], v[56:57], v[96:97]\n" "v_pk_add_f32 v[58:59], v[58:59], v[98:99]\n" "v_pk_add_f32 v[60:61], v[60:61], v[100:101]\n" "v_pk_add_f32 v[62:63], v[62:63], v[102:103]\n" "v_pk_add_f32 v[64:65], v[64:65], v[104:105]\n" "v_pk_add_f32 v[66:67], v[66:67], v[106:107]\n" "s_nop 1\n" "v_permlane32_swap_b32 v48, v50\n" "v_permlane32_swap_b32 v49, v51\n" "v_permlane32_swap_b32 v52, v54\n" "v_permlane32_swap_b32 v53, v55\n" "v_permlane32_swap_b32 v56, v58\n" "v_permlane32_swap_b32 v57, v59\n" "v_permlane32_swap_b32 v60, v62\n" "v_permlane32_swap_b32 v61, v63\n" "v_permlane32_swap_b32 v64, v66\n" "v_permlane32_swap_b32 v65, v67\n" "s_nop 1\n" "v_pk_add_f32 v[48:49], v[48:49], v[50:51]\n" "v_pk_add_f32 v[52:53], v[52:53], v[54:55]\n" "v_pk_add_f32 v[56:57], v[56:57], v[58:59]\n" "v_pk_add_f32 v[60:61], v[60:61], v[62:63]\n" "v_pk_add_f32 v[64:65], v[64:65], v[66:67]\n" "s_barrier\n" "ds_write_b64 %3, v[48:49] offset:0\n" "ds_write_b64 %3, v[52:53] offset:512\n" "ds_write_b64 %3, v[56:57] offset:1024\n" "ds_write_b64 %3, v[60:61] offset:1536\n" "ds_write_b64 %3, v[64:65] offset:2048\n" "s_waitcnt lgkmcnt(0)\n" "s_barrier\n", W_Y, ytag, wq_off, rq);
    CSTAMP(6 + 6 * n);
    resid_ln(xs + (b & 1) * 320, xm, true);
    REFRESH();
    float* red2 = (float*)(lds + C_RED2);
    {
      f32x4 d0, d1, d2;
      asm volatile(     "ds_read_b128 v[118:121], %3 offset:0\n" "ds_read_b128 v[94:97], %6 offset:0\n" "ds_read_b128 v[98:101], %6 offset:1024\n" "ds_read_b128 v[102:105], %6 offset:2048\n" "ds_read_b128 v[122:125], %3 offset:16\n" "ds_read_b128 v[106:109], %6 offset:3072\n" "ds_read_b128 v[110:113], %6 offset:4096\n" "ds_read_b128 v[114:117], %6 offset:5120\n" "s_waitcnt lgkmcnt(7)\n" "v_perm_b32 v126, v118, v118, %4\n" "v_perm_b32 v127, v118, v118, %5\n" "s_nop 1\n" "s_waitcnt lgkmcnt(4)\n" "v_mfma_f32_4x4x4_16b_bf16 %0, v[94:95], v[126:127], 0\n" "v_perm_b32 v128, v119, v119, %4\n" "v_mfma_f32_4x4x4_16b_bf16 %1, v[96:97], v[126:127], 0\n" "v_perm_b32 v129, v119, v119, %5\n" "v_mfma_f32_4x4x4_16b_bf16 %2, v[98:99], v[126:127], 0\n" "s_nop 0\n" "v_mfma_f32_4x4x4_16b_bf16 %0, v[100:101], v[128:129], %0\n" "v_perm_b32 v126, v120, v120, %4\n" "v_mfma_f32_4x4x4_16b_bf16 %1, v[102:103], v[128:129], %1\n" "v_perm_b32 v127, v120, v120, %5\n" "v_mfma_f32_4x4x4_16b_bf16 %2, v[104:105], v[128:129], %2\n" "s_nop 0\n" "ds_read_b128 v[94:97], %6 offset:6144\n" "ds_read_b128 v[98:101], %6 offset:7168\n" "ds_read_b128 v[102:105], %6 offset:8192\n" "s_waitcnt lgkmcnt(3)\n" "v_mfma_f32_4x4x4_16b_bf16 %0, v[106:107], v[126:127], %0\n" "v_perm_b32 v128, v121, v121, %4\n" "v_mfma_f32_4x4x4_16b_bf16 %1, v[108:109], v[126:127], %1\n" "v_perm_b32 v129, v121, v121, %5\n" "v_mfma_f32_4x4x4_16b_bf16 %2, v[110:111], v[126:127], %2\n" "s_nop 0\n" "v_mfma_f32_4x4x4_16b_bf16 %0, v[112:113], v[128:129], %0\n" "v_perm_b32 v126, v122, v122, %4\n" "v_mfma_f32_4x4x4_16b_bf16 %1, v[114:115], v[128:129], %1\n" "v_perm_b32 v127, v122, v122, %5\n" "v_mfma_f32_4x4x4_16b_bf16 %2, v[116:117], v[128:129], %2\n" "s_nop 0\n" "ds_read_b128 v[118:121], %3 offset:32\n" "ds_read_b128 v[106:109], %6 offset:9216\n" "ds_read_b128 v[110:113], %6 offset:10240\n" "ds_read_b128 v[114:117], %6 offset:11264\n" "s_waitcnt lgkmcnt(4)\n" "v_mfma_f32_4x4x4_16b_bf16 %0, v[94:95], v[126:127], %0\n" "v_perm_b32 v128, v123, v123, %4\n" "v_mfma_f32_4x4x4_16b_bf16 %1, v[96:97], v[126:127], %1\n" "v_perm_b32 v129, v123, v123, %5\n" "v_mfma_f32_4x4x4_16b_bf16 %2, v[98:99], v[126:127], %2\n" "s_nop 0\n" "v_mfma_f32_4x4x4_16b_bf16 %0, v[100:101], v[128:129], %0\n" "v_perm_b32 v126, v124, v124, %4\n" "v_mfma_f32_4x4x4_16b_bf16 %1, v[102:103], v[128:129], %1\n" "v_perm_b32 v127, v124, v124, %5\n" "v_mfma_f32_4x4x4_16b_bf16 %2, v[104:105], v[128:129], %2\n" "s_nop 0\n" "ds_read_b128 v[94:97], %6 offset:12288\n" "ds_read_b128 v[98:101], %6 offset:13312\n" "ds_read_b128 v[102:105], %6 offset:14336\n" "s_waitcnt lgkmcnt(3)\n" "v_mfma_f32_4x4x4_16b_bf16 %0, v[106:107], v[126:127], %0\n" "v_perm_b32 v128, v125, v125, %4\n" "v_mfma_f32_4x4x4_16b_bf16 %1, v[108:109], v[126:127], %1\n" "v_perm_b32 v129, v125, v125, %5\n" "v_mfma_f32_4x4x4_16b_bf16 %2, v[110:111], v[126:127], %2\n" "s_nop 0\n" "v_mfma_f32_4x4x4_16b_bf16 %0, v[112:113], v[128:129], %0\n" "v_perm_b32 v126, v118, v118, %4\n" "v_mfma_f32_4x4x4_16b_bf16 %1, v[114:115], v[128:129], %1\n" "v_perm_b32 v127, v118, v118, %5\n" "v_mfma_f32_4x4x4_16b_bf16 %2, v[116:117], v[128:129], %2\n" "s_nop 0\n" "ds_read_b128 v[122:125], %3 offset:48\n" "ds_read_b128 v[106:109], %6 offset:15360\n" "ds_read_b128 v[110:113], %6 offset:16384\n" "ds_read_b128 v[114:117], %6 offset:17408\n" "s_waitcnt lgkmcnt(4)\n" "v_mfma_f32_4x4x4_16b_bf16 %0, v[94:95], v[126:127], %0\n" "v_perm_b32 v128, v119, v119, %4\n" "v_mfma_f32_4x4x4_16b_bf16 %1, v[96:97], v[126:127], %1\n" "v_perm_b32 v129, v119, v119, %5\n" "v_mfma_f32_4x4x4_16b_bf16 %2, v[98:99], v[126:127], %2\n" "s_nop 0\n" "v_mfma_f32_4x4x4_16b_bf16 %0, v[100:101], v[128:129], %0\n" "v_perm_b32 v126, v120, v120, %4\n" "v_mfma_f32_4x4x4_16b_bf16 %1, v[102:103], v[128:129], %1\n" "v_perm_b32 v127, v120, v120, %5\n" "v_mfma_f32_4x4x4_16b_bf16 %2, v[104:105], v[128:129], %2\n" "s_nop 0\n" "ds_read_b128 v[94:97], %6 offset:18432\n" "ds_read_b128 v[98:101], %6 offset:19456\n" "ds_read_b128 v[102:105], %6 offset:20480\n" "s_waitcnt lgkmcnt(3)\n" "v_mfma_f32_4x4x4_16b_bf16 %0, v[106:107], v[126:127], %0\n" "v_perm_b32 v128, v121, v121, %4\n" "v_mfma_f32_4x4x4_16b_bf16 %1, v[108:109], v[126:127], %1\n" "v_perm_b32 v129, v121, v121, %5\n" "v_mfma_f32_4x4x4_16b_bf16 %2, v[110:111], v[126:127], %2\n" "s_nop 0\n" "v_mfma_f32_4x4x4_16b_bf16 %0, v[112:113], v[128:129], %0\n" "v_perm_b32 v126, v122, v122, %4\n" "v_mfma_f32_4x4x4_16b_bf16 %1, v[114:115], v[128:129], %1\n" "v_perm_b32 v127, v122, v122, %5\n" "v_mfma_f32_4x4x4_16b_bf16 %2, v[116:117], v[128:129], %2\n" "s_nop 0\n" "ds_read_b128 v[118:121], %3 offset:64\n" "ds_read_b128 v[106:109], %6 offset:21504\n" "ds_read_b128 v[110:113], %6 offset:22528\n" "ds_read_b128 v[114:117], %6 offset:23552\n" "s_waitcnt lgkmcnt(4)\n" "v_mfma_f32_4x4x4_16b_bf16 %0, v[94:95], v[126:127], %0\n" "v_perm_b32 v128, v123, v123, %4\n" "v_mfma_f32_4x4x4_16b_bf16 %1, v[96:97], v[126:127], %1\n" "v_perm_b32 v129, v123, v123, %5\n" "v_mfma_f32_4x4x4_16b_bf16 %2, v[98:99], v[126:127], %2\n" "s_nop 0\n" "v_mfma_f32_4x4x4_16b_bf16 %0, v[100:101], v[128:129], %0\n" "v_perm_b32 v126, v124, v124, %4\n" "v_mfma_f32_4x4x4_16b_bf16 %1, v[102:103], v[128:129], %1\n" "v_perm_b32 v127, v124, v124, %5\n" "v_mfma_f32_4x4x4_16b_bf16 %2, v[104:105], v[128:129], %2\n" "s_nop 0\n" "ds_read_b128 v[94:97], %6 offset:24576\n" "ds_read_b128 v[98:101], %6 offset:25600\n" "ds_read_b128 v[102:105], %6 offset:26624\n" "s_waitcnt lgkmcnt(3)\n" "v_mfma_f32_4x4x4_16b_bf16 %0, v[106:107], v[126:127], %0\n" "v_perm_b32 v128, v125, v125, %4\n" "v_mfma_f32_4x4x4_16b_bf16 %1, v[108:109], v[126:127], %1\n" "v_perm_b32 v129, v125, v125, %5\n" "v_mfma_f32_4x4x4_16b_bf16 %2, v[110:111], v[126:127], %2\n" "s_nop 0\n" "v_mfma_f32_4x4x4_16b_bf16 %0, v[112:113], v[128:129], %0\n" "v_perm_b32 v126, v118, v118, %4\n" "v_mfma_f32_4x4x4_16b_bf16 %1, v[114:115], v[128:129], %1\n" "v_perm_b32 v127, v118, v118, %5\n" "v_mfma_f32_4x4x4_16b_bf16 %2, v[116:117], v[128:129], %2\n" "s_nop 0\n" "ds_read_b128 v[106:109], %6 offset:27648\n" "ds_read_b128 v[110:113], %6 offset:28672\n" "ds_read_b128 v[114:117], %6 offset:29696\n" "s_waitcnt lgkmcnt(3)\n" "v_mfma_f32_4x4x4_16b_bf16 %0, v[94:95], v[126:127], %0\n" "v_perm_b32 v128, v119, v119, %4\n" "v_mfma_f32_4x4x4_16b_bf16 %1, v[96:97], v[126:127], %1\n" "v_perm_b32 v129, v119, v119, %5\n" "v_mfma_f32_4x4x4_16b_bf16 %2, v[98:99], v[126:127], %2\n" "s_nop 0\n" "v_mfma_f32_4x4x4_16b_bf16 %0, v[100:101], v[128:129], %0\n" "v_perm_b32 v126, v120, v120, %4\n" "v_mfma_f32_4x4x4_16b_bf16 %1, v[102:103], v[128:129], %1\n" "v_perm_b32 v127, v120, v120, %5\n" "v_mfma_f32_4x4x4_16b_bf16 %2, v[104:105], v[128:129], %2\n" "s_nop 0\n" "s_waitcnt lgkmcnt(0)\n" "v_mfma_f32_4x4x4_16b_bf16 %0, v[106:107], v[126:127], %0\n" "v_perm_b32 v128, v121, v121, %4\n" "v_mfma_f32_4x4x4_16b_bf16 %1, v[108:109], v[126:127], %1\n" "v_perm_b32 v129, v121, v121, %5\n" "v_mfma_f32_4x4x4_16b_bf16 %2, v[110:111], v[126:127], %2\n" "s_nop 0\n" "v_mfma_f32_4x4x4_16b_bf16 %0, v[112:113], v[128:129], %0\n" "v_mfma_f32_4x4x4_16b_bf16 %1, v[114:115], v[128:129], %1\n" "v_mfma_f32_4x4x4_16b_bf16 %2, v[116:117], v[128:129], %2\n" "s_nop 0\n" "s_nop 7\n" : "=&v"(d0), "=&v"(d1), "=&v"(d2) : "v"(ldsb + C_XN + 80 * (t >> 4)), "v"(sel0), "v"(sel1), "v"(ldsb + C_WGU + w * 30720 + lane * 16) : GV_CLOB);
      const f32x4 dg[3] = {d0, d1, d2};
      const int col = ((lane >> 3) & 1) * 48 + 6 * ((lane & 4) + (lane >> 4)) + (lane & 1);
#pragma unroll
      for (int c = 0; c < 3; ++c) {
        const float f = rs16(rs32(dg[c][0], dg[c][2]), rs32(dg[c][1], dg[c][3]));
        const float v = f + dpp<0x4E>(f);
        if ((lane & 2) == 0) red2[w * 96 + col + 2 * c] = v;
      }
    }
    __syncthreads();
    REFRESH();
    {
      const int c = 12 * w + 2 * (lane < 6 ? lane : 5);
      unsigned hb[2];
      asm volatile("" : "+v"(one));
#pragma unroll
      for (int e = 0; e < 2; ++e) {
        const float g = rbf((red2[c + e] + red2[96 + c + e]) + (red2[192 + c + e] + red2[288 + c + e]));
        const float u = rbf((red2[48 + c + e] + red2[144 + c + e]) + (red2[240 + c + e] + red2[336 + c + e]));
        const float z = 0.7978845608028654f * (g + 0.044715f * g * g * g);
        const float th = 1.f - 2.f * __builtin_amdgcn_rcpf(__expf(2.f * z) + 1.f);
        const float ge = rbf(0.5f * g * (one + th));
        hb[e] = f2bf(ge * u);
      }
      const unsigned pr = hb[0] | (hb[1] << 16);
      asm volatile(     "s_nop 0\n" "v_readlane_b32 s36, %5, 0\n" "v_readlane_b32 s37, %5, 1\n" "s_nop 0\n" "v_perm_b32 v126, s36, s36, %6\n" "v_perm_b32 v127, s36, s36, %7\n" "s_nop 1\n" "v_mfma_f32_4x4x4_16b_bf16 %0, v[200:201], v[126:127], 0\n" "v_readlane_b32 s38, %5, 2\n" "v_mfma_f32_4x4x4_16b_bf16 %1, v[202:203], v[126:127], 0\n" "v_perm_b32 v128, s37, s37, %6\n" "v_mfma_f32_4x4x4_16b_bf16 %2, v[204:205], v[126:127], 0\n" "v_perm_b32 v129, s37, s37, %7\n" "v_mfma_f32_4x4x4_16b_bf16 %3, v[206:207], v[126:127], 0\n" "v_mfma_f32_4x4x4_16b_bf16 %4, v[208:209], v[126:127], 0\n" "v_mfma_f32_4x4x4_16b_bf16 %0, v[210:211], v[128:129], %0\n" "v_readlane_b32 s39, %5, 3\n" "v_mfma_f32_4x4x4_16b_bf16 %1, v[212:213], v[128:129], %1\n" "v_perm_b32 v126, s38, s38, %6\n" "v_mfma_f32_4x4x4_16b_bf16 %2, v[214:215], v[128:129], %2\n" "v_perm_b32 v127, s38, s38, %7\n" "v_mfma_f32_4x4x4_16b_bf16 %3, v[216:217], v[128:129], %3\n" "v_mfma_f32_4x4x4_16b_bf16 %4, v[218:219], v[128:129], %4\n" "v_mfma_f32_4x4x4_16b_bf16 %0, v[220:221], v[126:127], %0\n" "v_readlane_b32 s36, %5, 4\n" "v_mfma_f32_4x4x4_16b_bf16 %1, v[222:223], v[126:127], %1\n" "v_perm_b32 v128, s39, s39, %6\n" "v_mfma_f32_4x4x4_16b_bf16 %2, v[224:225], v[126:127], %2\n" "v_perm_b32 v129, s39, s39, %7\n" "v_mfma_f32_4x4x4_16b_bf16 %3, v[226:227], v[126:127], %3\n" "v_mfma_f32_4x4x4_16b_bf16 %4, v[228:229], v[126:127], %4\n" "v_mfma_f32_4x4x4_16b_bf16 %0, v[230:231], v[128:129], %0\n" "v_readlane_b32 s37, %5, 5\n" "v_mfma_f32_4x4x4_16b_bf16 %1, v[232:233], v[128:129], %1\n" "v_perm_b32 v126, s36, s36, %6\n" "v_mfma_f32_4x4x4_16b_bf16 %2, v[234:235], v[128:129], %2\n" "v_perm_b32 v127, s36, s36, %7\n" "v_mfma_f32_4x4x4_16b_bf16 %3, v[236:237], v[128:129], %3\n" "v_mfma_f32_4x4x4_16b_bf16 %4, v[238:239], v[128:129], %4\n" "v_mfma_f32_4x4x4_16b_bf16 %0, v[240:241], v[126:127], %0\n" "v_perm_b32 v128, s37, s37, %6\n" "v_mfma_f32_4x4x4_16b_bf16 %1, v[242:243], v[126:127], %1\n" "v_perm_b32 v129, s37, s37, %7\n" "v_mfma_f32_4x4x4_16b_bf16 %2, v[244:245], v[126:127], %2\n" "v_mfma_f32_4x4x4_16b_bf16 %3, v[246:247], v[126:127], %3\n" "v_mfma_f32_4x4x4_16b_bf16 %4, v[248:249], v[126:127], %4\n" "v_mfma_f32_4x4x4_16b_bf16 %0, v[250:251], v[128:129], %0\n" "v_mfma_f32_4x4x4_16b_bf16 %1, v[252:253], v[128:129], %1\n" "v_mfma_f32_4x4x4_16b_bf16 %2, v[254:255], v[128:129], %2\n" "v_mfma_f32_4x4x4_16b_bf16 %3, a[250:251], v[128:129], %3\n" "v_mfma_f32_4x4x4_16b_bf16 %4, a[252:253], v[128:129], %4\n" "s_nop 7\n" : "=&v"(dd[0]), "=&v"(dd[1]), "=&v"(dd[2]), "=&v"(dd[3]), "=&v"(dd[4]) : "v"(pr), "v"(sel0), "v"(sel1) : GV_CLOB);
    }
    publish640(dd, W_Z, ytag);
    CSTAMP(7 + 6 * n);
    if (ps == 0) {
      GATHER(    "s_mov_b32 s36, 15\n" "s_mov_b32 s37, 0\n" "s_mov_b64 s[40:41], -1\n" "s_mov_b64 s[42:43], -1\n" "s_mov_b64 s[44:45], -1\n" "s_mov_b64 s[46:47], -1\n" "s_barrier\n" "s_cmp_eq_u32 %7, 0\n" "s_cbranch_scc0 800f\n" "s_mov_b32 s40, 0\n" "s_not_b64 exec, s[40:41]\n" "ds_read_b128 v[48:51], %8 offset:0\n" "ds_read_b128 v[52:55], %8 offset:512\n" "ds_read_b128 v[56:59], %8 offset:1024\n" "ds_read_b128 v[60:63], %8 offset:1536\n" "ds_read_b128 v[64:67], %8 offset:2048\n" "s_mov_b64 exec, -1\n" "800:\n" "s_cmp_eq_u32 %7, 4\n" "s_cbranch_scc0 801f\n" "s_mov_b32 s41, 0\n" "s_not_b64 exec, s[40:41]\n" "ds_read_b128 v[48:51], %8 offset:0\n" "ds_read_b128 v[52:55], %8 offset:512\n" "ds_read_b128 v[56:59], %8 offset:1024\n" "ds_read_b128 v[60:63], %8 offset:1536\n" "ds_read_b128 v[64:67], %8 offset:2048\n" "s_mov_b64 exec, -1\n" "801:\n" "s_cmp_eq_u32 %7, 1\n" "s_cbranch_scc0 810f\n" "s_mov_b32 s42, 0\n" "s_not_b64 exec, s[42:43]\n" "ds_read_b128 v[68:71], %8 offset:0\n" "ds_read_b128 v[72:75], %8 offset:512\n" "ds_read_b128 v[76:79], %8 offset:1024\n" "ds_read_b128 v[80:83], %8 offset:1536\n" "ds_read_b128 v[84:87], %8 offset:2048\n" "s_mov_b64 exec, -1\n" "810:\n" "s_cmp_eq_u32 %7, 5\n" "s_cbranch_scc0 811f\n" "s_mov_b32 s43, 0\n" "s_not_b64 exec, s[42:43]\n" "ds_read_b128 v[68:71], %8 offset:0\n" "ds_read_b128 v[72:75], %8 offset:512\n" "ds_read_b128 v[76:79], %8 offset:1024\n" "ds_read_b128 v[80:83], %8 offset:1536\n" "ds_read_b128 v[84:87], %8 offset:2048\n" "s_mov_b64 exec, -1\n" "811:\n" "s_cmp_eq_u32 %7, 2\n" "s_cbranch_scc0 820f\n" "s_mov_b32 s44, 0\n" "s_not_b64 exec, s[44:45]\n" "ds_read_b128 v[88:91], %8 offset:0\n" "ds_read_b128 v[92:95], %8 offset:512\n" "ds_read_b128 v[96:99], %8 offset:1024\n" "ds_read_b128 v[100:103], %8 offset:1536\n" "ds_read_b128 v[104:107], %8 offset:2048\n" "s_mov_b64 exec, -1\n" "820:\n" "s_cmp_eq_u32 %7, 6\n" "s_cbranch_scc0 821f\n" "s_mov_b32 s45, 0\n" "s_not_b64 exec, s[44:45]\n" "ds_read_b128 v[88:91], %8 offset:0\n" "ds_read_b128 v[92:95], %8 offset:512\n" "ds_read_b128 v[96:99], %8 offset:1024\n" "ds_read_b128 v[100:103], %8 offset:1536\n" "ds_read_b128 v[104:107], %8 offset:2048\n" "s_mov_b64 exec, -1\n" "821:\n" "s_cmp_eq_u32 %7, 3\n" "s_cbranch_scc0 830f\n" "s_mov_b32 s46, 0\n" "s_not_b64 exec, s[46:47]\n" "ds_read_b128 v[108:111], %8 offset:0\n" "ds_read_b128 v[112:115], %8 offset:512\n" "ds_read_b128 v[116:119], %8 offset:1024\n" "ds_read_b128 v[120:123], %8 offset:1536\n" "ds_read_b128 v[124:127], %8 offset:2048\n" "s_mov_b64 exec, -1\n" "830:\n" "s_cmp_eq_u32 %7, 7\n" "s_cbranch_scc0 831f\n" "s_mov_b32 s47, 0\n" "s_not_b64 exec, s[46:47]\n" "ds_read_b128 v[108:111], %8 offset:0\n" "ds_read_b128 v[112:115], %8 offset:512\n" "ds_read_b128 v[116:119], %8 offset:1024\n" "ds_read_b128 v[120:123], %8 offset:1536\n" "ds_read_b128 v[124:127], %8 offset:2048\n" "s_mov_b64 exec, -1\n" "831:\n" "s_sleep 16\n" "1:\n" "s_bitcmp1_b32 s36, 0\n" "s_cbranch_scc0 20f\n" "s_movk_i32 s38, 0\n" "s_mov_b64 exec, s[40:41]\n" "buffer_load_dwordx4 v[48:51], %0, %1, s38 offen offset:0 sc1\n" "buffer_load_dwordx4 v[52:55], %0, %1, s38 offen offset:512 sc1\n" "buffer_load_dwordx4 v[56:59], %0, %1, s38 offen offset:1024 sc1\n" "buffer_load_dwordx4 v[60:63], %0, %1, s38 offen offset:1536 sc1\n" "buffer_load_dwordx4 v[64:67], %0, %1, s38 offen offset:2048 sc1\n" "20:\n" "s_bitcmp1_b32 s36, 1\n" "s_cbranch_scc0 21f\n" "s_movk_i32 s38, 2560\n" "s_mov_b64 exec, s[42:43]\n" "buffer_load_dwordx4 v[68:71], %0, %1, s38 offen offset:0 sc1\n" "buffer_load_dwordx4 v[72:75], %0, %1, s38 offen offset:512 sc1\n" "buffer_load_dwordx4 v[76:79], %0, %1, s38 offen offset:1024 sc1\n" "buffer_load_dwordx4 v[80:83], %0, %1, s38 offen offset:1536 sc1\n" "buffer_load_dwordx4 v[84:87], %0, %1, s38 offen offset:2048 sc1\n" "21:\n" "s_bitcmp1_b32 s36, 2\n" "s_cbranch_scc0 22f\n" "s_movk_i32 s38, 5120\n" "s_mov_b64 exec, s[44:45]\n" "buffer_load_dwordx4 v[88:91], %0, %1, s38 offen offset:0 sc1\n" "buffer_load_dwordx4 v[92:95], %0, %1, s38 offen offset:512 sc1\n" "buffer_load_dwordx4 v[96:99], %0, %1, s38 offen offset:1024 sc1\n" "buffer_load_dwordx4 v[100:103], %0, %1, s38 offen offset:1536 sc1\n" "buffer_load_dwordx4 v[104:107], %0, %1, s38 offen offset:2048 sc1\n" "22:\n" "s_bitcmp1_b32 s36, 3\n" "s_cbranch_scc0 23f\n" "s_movk_i32 s38, 7680\n" "s_mov_b64 exec, s[46:47]\n" "buffer_load_dwordx4 v[108:111], %0, %1, s38 offen offset:0 sc1\n" "buffer_load_dwordx4 v[112:115], %0, %1, s38 offen offset:512 sc1\n" "buffer_load_dwordx4 v[116:119], %0, %1, s38 offen offset:1024 sc1\n" "buffer_load_dwordx4 v[120:123], %0, %1, s38 offen offset:1536 sc1\n" "buffer_load_dwordx4 v[124:127], %0, %1, s38 offen offset:2048 sc1\n" "23:\n" "s_mov_b64 exec, -1\n" "s_waitcnt vmcnt(0)\n" "5:\n" "s_waitcnt lgkmcnt(0)\n" "s_bitcmp1_b32 s36, 0\n" "s_cbranch_scc0 30f\n" "v_bitop3_b32 v128, v48, v49, v50 bitop3:0x80\n" "v_bitop3_b32 v129, v51, v52, v53 bitop3:0x80\n" "v_bitop3_b32 v128, v128, v54, v55 bitop3:0x80\n" "v_bitop3_b32 v129, v129, v56, v57 bitop3:0x80\n" "v_bitop3_b32 v128, v128, v58, v59 bitop3:0x80\n" "v_bitop3_b32 v129, v129, v60, v61 bitop3:0x80\n" "v_bitop3_b32 v128, v128, v62, v63 bitop3:0x80\n" "v_bitop3_b32 v129, v129, v64, v65 bitop3:0x80\n" "v_bitop3_b32 v128, v128, v66, v67 bitop3:0x80\n" "v_bitop3_b32 v128, v128, v129, %2 bitop3:0x80\n" "v_cmp_eq_u32 vcc, 0, v128\n" "s_or_b64 s[40:41], vcc, vcc\n" "s_cbranch_scc1 30f\n" "s_bitset0_b32 s36, 0\n" "30:\n" "s_bitcmp1_b32 s36, 1\n" "s_cbranch_scc0 31f\n" "v_bitop3_b32 v128, v68, v69, v70 bitop3:0x80\n" "v_bitop3_b32 v129, v71, v72, v73 bitop3:0x80\n" "v_bitop3_b32 v128, v128, v74, v75 bitop3:0x80\n" "v_bitop3_b32 v129, v129, v76, v77 bitop3:0x80\n" "v_bitop3_b32 v128, v128, v78, v79 bitop3:0x80\n" "v_bitop3_b32 v129, v129, v80, v81 bitop3:0x80\n" "v_bitop3_b32 v128, v128, v82, v83 bitop3:0x80\n" "v_bitop3_b32 v129, v129, v84, v85 bitop3:0x80\n" "v_bitop3_b32 v128, v128, v86, v87 bitop3:0x80\n" "v_bitop3_b32 v128, v128, v129, %2 bitop3:0x80\n" "v_cmp_eq_u32 vcc, 0, v128\n" "s_or_b64 s[42:43], vcc, vcc\n" "s_cbranch_scc1 31f\n" "s_bitset0_b32 s36, 1\n" "31:\n" "s_bitcmp1_b32 s36, 2\n" "s_cbranch_scc0 32f\n" "v_bitop3_b32 v128, v88, v89, v90 bitop3:0x80\n" "v_bitop3_b32 v129, v91, v92, v93 bitop3:0x80\n" "v_bitop3_b32 v128, v128, v94, v95 bitop3:0x80\n" "v_bitop3_b32 v129, v129, v96, v97 bitop3:0x80\n" "v_bitop3_b32 v128, v128, v98, v99 bitop3:0x80\n" "v_bitop3_b32 v129, v129, v100, v101 bitop3:0x80\n" "v_bitop3_b32 v128, v128, v102, v103 bitop3:0x80\n" "v_bitop3_b32 v129, v129, v104, v105 bitop3:0x80\n" "v_bitop3_b32 v128, v128, v106, v107 bitop3:0x80\n" "v_bitop3_b32 v128, v128, v129, %2 bitop3:0x80\n" "v_cmp_eq_u32 vcc, 0, v128\n" "s_or_b64 s[44:45], vcc, vcc\n" "s_cbranch_scc1 32f\n" "s_bitset0_b32 s36, 2\n" "32:\n" "s_bitcmp1_b32 s36, 3\n" "s_cbranch_scc0 33f\n" "v_bitop3_b32 v128, v108, v109, v110 bitop3:0x80\n" "v_bitop3_b32 v129, v111, v112, v113 bitop3:0x80\n" "v_bitop3_b32 v128, v128, v114, v115 bitop3:0x80\n" "v_bitop3_b32 v129, v129, v116, v117 bitop3:0x80\n" "v_bitop3_b32 v128, v128, v118, v119 bitop3:0x80\n" "v_bitop3_b32 v129, v129, v120, v121 bitop3:0x80\n" "v_bitop3_b32 v128, v128, v122, v123 bitop3:0x80\n" "v_bitop3_b32 v129, v129, v124, v125 bitop3:0x80\n" "v_bitop3_b32 v128, v128, v126, v127 bitop3:0x80\n" "v_bitop3_b32 v128, v128, v129, %2 bitop3:0x80\n" "v_cmp_eq_u32 vcc, 0, v128\n" "s_or_b64 s[46:47], vcc, vcc\n" "s_cbranch_scc1 33f\n" "s_bitset0_b32 s36, 3\n" "33:\n" "s_cmp_eq_u32 s36, 0\n" "s_cbranch_scc1 4f\n" "6:\n" "s_add_u32 s37, s37, 1\n" "s_cmp_lt_u32 s37, 131072\n" "s_cbranch_scc0 7f\n" "s_branch 1b\n" "buffer_load_dword v128, %6, %1, 0 offen sc1\n" "s_waitcnt vmcnt(0)\n" "v_and_b32 v128, %2, v128\n" "v_cmp_ne_u32 vcc, 0, v128\n" "s_cmp_eq_u64 vcc, -1\n" "s_cbranch_scc1 1b\n" "s_branch 6b\n" "7:\n" "v_mov_b32 v128, 0x3e8\n" "v_mov_b32 v129, 0\n" "buffer_store_dword v128, v129, %1, 0 offen sc1\n" "4:\n" "v_pk_add_f32 v[48:49], v[48:49], v[68:69]\n" "v_pk_add_f32 v[88:89], v[88:89], v[108:109]\n" "v_pk_add_f32 v[50:51], v[50:51], v[70:71]\n" "v_pk_add_f32 v[90:91], v[90:91], v[110:111]\n" "v_pk_add_f32 v[52:53], v[52:53], v[72:73]\n" "v_pk_add_f32 v[92:93], v[92:93], v[112:113]\n" "v_pk_add_f32 v[54:55], v[54:55], v[74:75]\n" "v_pk_add_f32 v[94:95], v[94:95], v[114:115]\n" "v_pk_add_f32 v[56:57], v[56:57], v[76:77]\n" "v_pk_add_f32 v[96:97], v[96:97], v[116:117]\n" "v_pk_add_f32 v[58:59], v[58:59], v[78:79]\n" "v_pk_add_f32 v[98:99], v[98:99], v[118:119]\n" "v_pk_add_f32 v[60:61], v[60:61], v[80:81]\n" "v_pk_add_f32 v[100:101], v[100:101], v[120:121]\n" "v_pk_add_f32 v[62:63], v[62:63], v[82:83]\n" "v_pk_add_f32 v[102:103], v[102:103], v[122:123]\n" "v_pk_add_f32 v[64:65], v[64:65], v[84:85]\n" "v_pk_add_f32 v[104:105], v[104:105], v[124:125]\n" "v_pk_add_f32 v[66:67], v[66:67], v[86:87]\n" "v_pk_add_f32 v[106:107], v[106:107], v[126:127]\n" "v_pk_add_f32 v[48:49], v[48:49], v[88:89]\n" "v_pk_add_f32 v[50:51], v[50:51], v[90:91]\n" "v_pk_add_f32 v[52:53], v[52:53], v[92:93]\n" "v_pk_add_f32 v[54:55], v[54:55], v[94:95]\n" "v_pk_add_f32 v[56:57], v[56:57], v[96:97]\n" "v_pk_add_f32 v[58:59], v[58:59], v[98:99]\n" "v_pk_add_f32 v[60:61], v[60:61], v[100:101]\n" "v_pk_add_f32 v[62:63], v[62:63], v[102:103]\n" "v_pk_add_f32 v[64:65], v[64:65], v[104:105]\n" "v_pk_add_f32 v[66:67], v[66:67], v[106:107]\n" "s_nop 1\n" "v_permlane32_swap_b32 v48, v50\n" "v_permlane32_swap_b32 v49, v51\n" "v_permlane32_swap_b32 v52, v54\n" "v_permlane32_swap_b32 v53, v55\n" "v_permlane32_swap_b32 v56, v58\n" "v_permlane32_swap_b32 v57, v59\n" "v_permlane32_swap_b32 v60, v62\n" "v_permlane32_swap_b32 v61, v63\n" "v_permlane32_swap_b32 v64, v66\n" "v_permlane32_swap_b32 v65, v67\n" "s_nop 1\n" "v_pk_add_f32 v[48:49], v[48:49], v[50:51]\n" "v_pk_add_f32 v[52:53], v[52:53], v[54:55]\n" "v_pk_add_f32 v[56:57], v[56:57], v[58:59]\n" "v_pk_add_f32 v[60:61], v[60:61], v[62:63]\n" "v_pk_add_f32 v[64:65], v[64:65], v[66:67]\n" "s_barrier\n" "ds_write_b64 %3, v[48:49] offset:0\n" "ds_write_b64 %3, v[52:53] offset:512\n" "ds_write_b64 %3, v[56:57] offset:1024\n" "ds_write_b64 %3, v[60:61] offset:1536\n" "ds_write_b64 %3, v[64:65] offset:2048\n" "s_waitcnt lgkmcnt(0)\n" "s_barrier\n", W_Z, ytag, wq_off, rq);
      CSTAMP(8 + 6 * n);
      resid_ln(xm, xs + (b & 1) * 320, true);
      qproj(4 + b, C_XN);
      REFRESH();
      LOAD_WO_T();
    } else {
      REFRESH();
      const int pq = t < 160 ? t : 159, pp = pq / 5, qd = pq % 5;
      v4u z; unsigned spins = 0;
      for (;;) {
        asm volatile("" ::: "memory");
        z = ld16(rw, W_Z + pp * 2560 + j * 80 + qd * 16);
        if (__all((z.x & z.y & z.z & z.w & ytag) != 0u)) break;
        if (spin_fail(spins, rw, 4000 + n)) break;
      }
      __syncthreads();
      if (t < 160) *(v4u*)(red + pp * 20 + qd * 4) = z;
      __syncthreads();
      CSTAMP(8 + 6 * n);
      if (t < 20) {
        float s = 0.f;
#pragma unroll
        for (int p = 0; p < 32; p += 4) s += (red[p * 20 + t] + red[p * 20 + 20 + t]) + (red[p * 20 + 40 + t] + red[p * 20 + 60 + t]);
        const int pos = j * 20 + t;
        const float xo = bflo(((const unsigned short*)xm)[pos]);
        a.out[b * 640 + ((pos & ~3) | ((pos & 1) << 1) | ((pos >> 1) & 1))] = (unsigned short)f2bf(xo + rbf(s));
      }
      __syncthreads();
    }
    REFRESH();
    if (n < 7) LOAD_WO_Z();
    CSTAMP(9 + 6 * n);
  }
}
}

__global__ void __launch_bounds__(256, 1) __attribute__((amdgpu_num_vgpr(48))) fused_decode(Args a) {
  extern __shared__ __attribute__((aligned(16))) char lds[];
  const int g8 = blockIdx.x & 7, rank = blockIdx.x >> 3;
  if (rank >= 28) { chain_main(a, lds, (rank - 28) * 8 + g8); return; }
  const int t = threadIdx.x, lane = t & 63, w = __builtin_amdgcn_readfirstlane(t >> 6);
  const rsrc_t rw = __builtin_amdgcn_make_buffer_rsrc((void*)a.ws, 0, (int)W_END, 0x00020000);
  const int P = (g8 & 3) * 28 + rank, side = g8 >> 2;
  const int sc = P * 2 + side;
  const unsigned myiod = xcc_id() >> 2;
#define PSTAMP(id) do {} while (0)
  PSTAMP(40);
  PSTAMP(41);
  unsigned char* c2 = (unsigned char*)(lds + L_KSLOT);
  int* vts = (int*)(lds + L_KSLOT + 1024);
  if (side == 0) {
    const v2u e0 = ld8(rw, W_PROBE + t * 8), e1 = ld8(rw, W_PROBE + (t + 256 < 2 * NPG ? t + 256 : 0) * 8);
    auto clampv = [](unsigned v) { const int x = (int)v; return x > 300 ? 300 : (x < -300 ? -300 : x); };
    vts[8 + t] = clampv(e0.x) + clampv(e0.y);
    if (t + 256 < 2 * NPG) vts[264 + t] = clampv(e1.x) + clampv(e1.y);
    if (t < 8) { vts[t] = 0; vts[2 * NPG + 8 + t] = 0; }
    __syncthreads();
#pragma unroll
    for (int i = 0; i < 2; ++i) { const int s = t + 256 * i;
      if (s < 2 * NPG) { const int* v = vts + 8 + s;
        int l = 0, r = 0;
#pragma unroll
        for (int d = 1; d <= 6; ++d) { l += (d & 1) ? -v[-d] : v[-d]; r += (d & 1) ? -v[d] : v[d]; }
        if (l == 0) l = r;
        if (r == 0) r = l;
        const bool run = (l > 900 && r > 900) || (l < -900 && r < -900);
        c2[s] = ((run ? l : 3 * v[0] - v[-1] - v[1]) > 0) ? 0 : 1; } }
    __syncthreads();
  }
  PSTAMP(42);
  const int m = P / 7, k7 = P % 7;
  const int iR = k7 >= 4 ? k7 - 4 : k7, hR = 2 * m + (k7 >= 4), uR = 21 * iR, nR = k7 == 3 ? 20 : 21;
  const int hN = 2 * m + (k7 >= 3);
  const int uN = k7 < 3 ? 83 + 15 * k7 : (k7 == 3 ? 63 : 80 + 16 * (k7 - 4)), nN = k7 < 3 ? 15 : (k7 == 3 ? 17 : 16), iN = k7 == 3 ? 3 : iR;
  {
    unsigned* desc = (unsigned*)(lds + L_DESC);
    const bool isR = lane < NR;
    int i = isR ? lane : lane - NR;
    const bool pad = i >= (isR ? nR : nN);
    if (pad) i = (isR ? nR : nN) - 1;
    const int U = (isR ? uR : uN) + i, h = isR ? hR : hN;
    const int cls4 = U & 3, u = U >> 2;
    const unsigned sb = (unsigned)(w * NH + h) * HEAD_BYTES + (unsigned)u * 98304u;
    const int cbase = (cls4 & 1) | ((cls4 >> 1) << 2);
    unsigned cc[2];
    if (side == 0) {
#pragma unroll
      for (int T = 0; T < 2; ++T) {
        const unsigned long long base = (unsigned long long)(T ? a.v : a.k);
        const unsigned long long ad = base + sb + (unsigned)(cbase + 8) * 4096u;
        const unsigned iod = ((unsigned)(ad >> 13) & 1u) ^ c2[T * NPG + (int)((ad >> 21) - (base >> 21))];
        cc[T] = (unsigned)cbase | (iod == (myiod ^ 0u) ? 0u : 2u);
      }
      {
        const unsigned long long d0 = __ballot(cc[0] != cc[1] && !pad);
        if (__popcll(isR ? (d0 & ((1ull << NR) - 1)) : (d0 >> NR)) <= 4) cc[1] = cc[0];
        if (isR == (1 == 1)) { if (((lane >> 2) ^ lane) & 1) cc[1] = cc[0]; else cc[0] = cc[1]; }
      }
      const unsigned long long mk = __ballot(cc[0] & 2u), mv = __ballot(cc[1] & 2u);
      if (lane == 0) st16(rw, W_HS + P * 64 + w * 16, v4u{((unsigned)mk & 0xfffffu) | 0x80000000u, ((unsigned)(mk >> 20) & 0xfffffu) | 0x80000000u,
                                                         ((unsigned)mv & 0xfffffu) | 0x80000000u, ((unsigned)(mv >> 20) & 0xfffffu) | 0x80000000u});
    } else {
      v4u hs; unsigned spins = 0;
      for (;;) {
        asm volatile("" ::: "memory");
        hs = ld16(rw, W_HS + P * 64 + w * 16);
        if ((hs.x & hs.y & hs.z & hs.w) >> 31) break;
        if (++spins > SPIN_CAP) { st4(rw, W_ERR, 500); break; }
      }
      const int lc = lane < 40 ? lane : 39;
      cc[0] = (unsigned)cbase | ((((lc < 20 ? hs.x >> lc : hs.y >> (lc - 20)) & 1u) ^ 1u) << 1);
      cc[1] = (unsigned)cbase | ((((lc < 20 ? hs.z >> lc : hs.w >> (lc - 20)) & 1u) ^ 1u) << 1);
    }
    const unsigned long long dm = __ballot(cc[0] != cc[1]);
    unsigned dk = sb + (cc[0] / 3) * 12288u + cc[0] % 3, dv = sb + (cc[1] / 3) * 12288u + cc[1] % 3;
    if (pad) { dk = 0xf0000000u; dv = 0xf0000000u; }
    if (lane == 40) dk = (unsigned)(dm & ((1ull << NR) - 1));
    if (lane == 41) dk = (unsigned)((dm >> NR) & ((1ull << NN) - 1));
    __syncthreads();
    if (w == 0) ((unsigned*)(lds + L_CU + 2304))[lane] = 0u;
    desc[(w * 2) * 64 + lane] = dk;
    desc[(w * 2 + 1) * 64 + lane] = dv;
    unsigned* pat = (unsigned*)(lds + L_MISC + w * WAVE_MISC);
#pragma unroll
    for (int c0 = 0; c0 < 3; ++c0) pat[c0 * 64 + lane] = (unsigned)(class_row(c0, 8 * w + (lane >> 3)) * 384 + (lane & 7) * 16);
  }
  __syncthreads();
  PSTAMP(43);
  const unsigned ldsb = (unsigned)(unsigned long long)(__attribute__((address_space(3))) char*)lds;
  const unsigned xin = W_X + (unsigned)sc * XWAVE + w * 288, xout = W_X + (unsigned)(sc ^ 1) * XWAVE;
  const unsigned pr = W_PART + hR * PART_HEAD + (iR * 2 + side) * PART_SLOT, pn = W_PART + hN * PART_HEAD + (iN * 2 + side) * PART_SLOT;
  const unsigned qr = W_Q + hR * 1024, qn = W_Q + hN * 1024;
  asm volatile(
    "s_mov_b32 s36, %0\n s_mov_b32 s37, %1\n s_mov_b32 s38, 0x18000000\n s_mov_b32 s39, 0x20000\n"
    "s_mov_b32 s40, %2\n s_mov_b32 s41, %3\n s_mov_b32 s42, 0x18000000\n s_mov_b32 s43, 0x20000\n"
    "s_mov_b32 s44, %4\n s_mov_b32 s45, %5\n s_mov_b32 s46, %6\n s_mov_b32 s47, 0x20000\n"
    "s_mov_b32 s48, %7\n s_mov_b32 s49, %8\n s_mov_b32 s50, %9\n s_mov_b32 s51, %10\n s_mov_b32 s52, %11\n s_mov_b32 s53, %12\n"
    "s_mov_b32 s54, %13\n s_mov_b32 s55, %14\n s_mov_b32 s56, %15\n s_mov_b32 s74, %16\n s_mov_b32 s75, %17\n s_mov_b32 s78, %18\n s_mov_b32 s79, %19\n s_mov_b32 s63, %20\n s_mov_b32 s88, %21\n s_mov_b32 s89, %22\n"
    "s_mov_b32 s71, 0x07060302\n s_mov_b32 s57, 0x3dd53b95\n s_mov_b32 s68, 0xaaaaaaaa\n s_mov_b32 s69, 0xaaaaaaaa\n"
    "v_mbcnt_lo_u32_b32 v248, -1, 0\n v_mbcnt_hi_u32_b32 v248, -1, v248\n"
    "v_lshlrev_b32 v240, 4, v248\n v_lshlrev_b32 v239, 2, v248\n v_add_u32 v239, s74, v239\n"
    "v_lshrrev_b32 v244, 3, v248\n v_lshlrev_b32 v241, 2, v244\n v_add_u32 v241, s74, v241\n"
    "v_and_b32 v249, 7, v248\n v_lshlrev_b32 v245, 4, v249\n v_add_u32 v245, s74, v245\n v_add_u32 v245, 2304, v245\n"
    "v_add_u32 v247, s54, v240\n v_and_b32 v221, 4, v248\n v_lshlrev_b32 v221, 3, v221\n v_add_u32 v221, v221, v241\n"
    "v_lshlrev_b32 v218, 1, v244\n v_add_u32 v218, s74, v218\n v_lshlrev_b32 v219, 1, v248\n v_add_u32 v219, s63, v219\n v_lshlrev_b32 v220, 3, v248\n v_add_u32 v220, s63, v220\n"
    "v_and_b32 v249, 3, v248\n v_mov_b32 v250, 0x0c0c0100\n v_mov_b32 v251, 0x01000c0c\n v_mov_b32 v252, 0x0c0c0c0c\n"
    "v_cmp_eq_u32 vcc, 0, v249\n v_cmp_eq_u32 s[64:65], 1, v249\n s_nop 1\n v_cndmask_b32 v242, v252, v250, vcc\n v_cndmask_b32 v242, v242, v251, s[64:65]\n"
    "v_cmp_eq_u32 vcc, 2, v249\n v_cmp_eq_u32 s[64:65], 3, v249\n s_nop 1\n v_cndmask_b32 v243, v252, v250, vcc\n v_cndmask_b32 v243, v243, v251, s[64:65]\n"
    "ds_read_b32 v230, v239\n ds_read_b32 v231, v239 offset:256\n ds_read_b32 v232, v239 offset:512\n s_waitcnt lgkmcnt(0)\n"
R"ASM(.set SPINCAP, 131072
.set NRC, 21
.set QSTAGEC, 2304
.set PFC, 1792
.set STMPC, 3840
.set WMISC, 3968

.macro ISSUE_A dreg, ln
  v_readlane_b32 s60, v[\dreg], \ln
  s_and_b32 s61, s60, 3
  s_andn2_b32 s62, s60, 3
  s_set_gpr_idx_on s61, gpr_idx(SRC0)
  v_mov_b32 v246, v230
  s_set_gpr_idx_off
.endm
.macro LDV dreg, ln, rs, d, pol
  ISSUE_A \dreg, \ln
  buffer_load_dwordx4 v[\d:\d+3], v246, s[\rs:\rs+3], s62 offen \pol
  buffer_load_dwordx4 v[\d+4:\d+7], v246, s[\rs:\rs+3], s62 offen offset:128 \pol
  buffer_load_dwordx4 v[\d+8:\d+11], v246, s[\rs:\rs+3], s62 offen offset:256 \pol
.endm
.macro LDA dreg, ln, rs, d, pol
  ISSUE_A \dreg, \ln
  buffer_load_dwordx4 a[\d:\d+3], v246, s[\rs:\rs+3], s62 offen \pol
  buffer_load_dwordx4 a[\d+4:\d+7], v246, s[\rs:\rs+3], s62 offen offset:128 \pol
  buffer_load_dwordx4 a[\d+8:\d+11], v246, s[\rs:\rs+3], s62 offen offset:256 \pol
.endm
.macro LDL dreg, ln, rs, off, pol
  ISSUE_A \dreg, \ln
  s_add_u32 m0, s54, \off
  s_nop 0
  buffer_load_dwordx4 v246, s[\rs:\rs+3], s62 offen \pol lds
  s_add_u32 m0, s54, \off+896
  s_nop 0
  buffer_load_dwordx4 v246, s[\rs:\rs+3], s62 offen offset:128 \pol lds
  s_add_u32 m0, s54, \off+1792
  s_nop 0
  buffer_load_dwordx4 v246, s[\rs:\rs+3], s62 offen offset:256 \pol lds
.endm
.macro L2V d, off
  ds_read_b128 v[\d:\d+3], v247 offset:\off
  ds_read_b128 v[\d+4:\d+7], v247 offset:\off+1024
  ds_read_b128 v[\d+8:\d+11], v247 offset:\off+2048
.endm
.macro KC d, q, so
  v_dot2_f32_bf16 v248, v[\d], v[\q], 0
  v_dot2_f32_bf16 v249, v[\d+6], v[\q+6], 0
  v_dot2_f32_bf16 v248, v[\d+1], v[\q+1], v248
  v_dot2_f32_bf16 v249, v[\d+7], v[\q+7], v249
  v_dot2_f32_bf16 v248, v[\d+2], v[\q+2], v248
  v_dot2_f32_bf16 v249, v[\d+8], v[\q+8], v249
  v_dot2_f32_bf16 v248, v[\d+3], v[\q+3], v248
  v_dot2_f32_bf16 v249, v[\d+9], v[\q+9], v249
  v_dot2_f32_bf16 v248, v[\d+4], v[\q+4], v248
  v_dot2_f32_bf16 v249, v[\d+10], v[\q+10], v249
  v_dot2_f32_bf16 v248, v[\d+5], v[\q+5], v248
  v_dot2_f32_bf16 v249, v[\d+11], v[\q+11], v249
  s_nop 2
  v_add_f32 v248, v248, v249
  s_nop 1
  v_add_f32_dpp v248, v248, v248 quad_perm:[1,0,3,2] row_mask:0xf bank_mask:0xf
  s_nop 1
  v_add_f32_dpp v248, v248, v248 quad_perm:[2,3,0,1] row_mask:0xf bank_mask:0xf
  s_nop 1
  v_add_f32_dpp v248, v248, v248 row_half_mirror row_mask:0xf bank_mask:0xf
  ds_write_b32 v241, v248 offset:\so
.endm
.macro KC2 d, e, q, so
  v_dot2_f32_bf16 v248, v[\d+0], v[\q+0], 0
  v_dot2_f32_bf16 v249, v[\d+6], v[\q+6], 0
  v_dot2_f32_bf16 v250, v[\e+0], v[\q+0], 0
  v_dot2_f32_bf16 v251, v[\e+6], v[\q+6], 0
  v_dot2_f32_bf16 v248, v[\d+1], v[\q+1], v248
  v_dot2_f32_bf16 v249, v[\d+7], v[\q+7], v249
  v_dot2_f32_bf16 v250, v[\e+1], v[\q+1], v250
  v_dot2_f32_bf16 v251, v[\e+7], v[\q+7], v251
  v_dot2_f32_bf16 v248, v[\d+2], v[\q+2], v248
  v_dot2_f32_bf16 v249, v[\d+8], v[\q+8], v249
  v_dot2_f32_bf16 v250, v[\e+2], v[\q+2], v250
  v_dot2_f32_bf16 v251, v[\e+8], v[\q+8], v251
  v_dot2_f32_bf16 v248, v[\d+3], v[\q+3], v248
  v_dot2_f32_bf16 v249, v[\d+9], v[\q+9], v249
  v_dot2_f32_bf16 v250, v[\e+3], v[\q+3], v250
  v_dot2_f32_bf16 v251, v[\e+9], v[\q+9], v251
  v_dot2_f32_bf16 v248, v[\d+4], v[\q+4], v248
  v_dot2_f32_bf16 v249, v[\d+10], v[\q+10], v249
  v_dot2_f32_bf16 v250, v[\e+4], v[\q+4], v250
  v_dot2_f32_bf16 v251, v[\e+10], v[\q+10], v251
  v_dot2_f32_bf16 v248, v[\d+5], v[\q+5], v248
  v_dot2_f32_bf16 v249, v[\d+11], v[\q+11], v249
  v_dot2_f32_bf16 v250, v[\e+5], v[\q+5], v250
  v_dot2_f32_bf16 v251, v[\e+11], v[\q+11], v251
  s_nop 0
  v_add_f32 v248, v248, v249
  s_nop 0
  v_add_f32 v250, v250, v251
  s_nop 0
  v_add_f32_dpp v249, v248, v248 row_half_mirror row_mask:0xf bank_mask:0x5
  v_add_f32_dpp v249, v250, v250 row_half_mirror row_mask:0xf bank_mask:0xa
  s_nop 1
  v_add_f32_dpp v249, v249, v249 quad_perm:[1,0,3,2] row_mask:0xf bank_mask:0xf
  s_nop 1
  v_add_f32_dpp v249, v249, v249 quad_perm:[2,3,0,1] row_mask:0xf bank_mask:0xf
  ds_write_b32 v221, v249 offset:\so
.endm
.macro PPRD so
  ds_read_u16 v250, v218 offset:\so
.endm
.macro VPRE
  s_waitcnt lgkmcnt(0)
  v_perm_b32 v252, v250, v250, v242
  v_perm_b32 v253, v250, v250, v243
.endm
.macro VC0V d
  v_mfma_f32_4x4x4_16b_bf16 v[192:195], v[\d:\d+1], v[252:253], 0
  v_mfma_f32_4x4x4_16b_bf16 v[196:199], v[\d+2:\d+3], v[252:253], 0
  v_mfma_f32_4x4x4_16b_bf16 v[200:203], v[\d+4:\d+5], v[252:253], 0
  v_mfma_f32_4x4x4_16b_bf16 v[204:207], v[\d+6:\d+7], v[252:253], 0
  v_mfma_f32_4x4x4_16b_bf16 v[208:211], v[\d+8:\d+9], v[252:253], 0
  v_mfma_f32_4x4x4_16b_bf16 v[212:215], v[\d+10:\d+11], v[252:253], 0
.endm
.macro VCV d
  v_mfma_f32_4x4x4_16b_bf16 v[192:195], v[\d:\d+1], v[252:253], v[192:195]
  v_mfma_f32_4x4x4_16b_bf16 v[196:199], v[\d+2:\d+3], v[252:253], v[196:199]
  v_mfma_f32_4x4x4_16b_bf16 v[200:203], v[\d+4:\d+5], v[252:253], v[200:203]
  v_mfma_f32_4x4x4_16b_bf16 v[204:207], v[\d+6:\d+7], v[252:253], v[204:207]
  v_mfma_f32_4x4x4_16b_bf16 v[208:211], v[\d+8:\d+9], v[252:253], v[208:211]
  v_mfma_f32_4x4x4_16b_bf16 v[212:215], v[\d+10:\d+11], v[252:253], v[212:215]
.endm
.macro VC0A d
  v_mfma_f32_4x4x4_16b_bf16 v[192:195], a[\d:\d+1], v[252:253], 0
  v_mfma_f32_4x4x4_16b_bf16 v[196:199], a[\d+2:\d+3], v[252:253], 0
  v_mfma_f32_4x4x4_16b_bf16 v[200:203], a[\d+4:\d+5], v[252:253], 0
  v_mfma_f32_4x4x4_16b_bf16 v[204:207], a[\d+6:\d+7], v[252:253], 0
  v_mfma_f32_4x4x4_16b_bf16 v[208:211], a[\d+8:\d+9], v[252:253], 0
  v_mfma_f32_4x4x4_16b_bf16 v[212:215], a[\d+10:\d+11], v[252:253], 0
.endm
.macro VCA d
  v_mfma_f32_4x4x4_16b_bf16 v[192:195], a[\d:\d+1], v[252:253], v[192:195]
  v_mfma_f32_4x4x4_16b_bf16 v[196:199], a[\d+2:\d+3], v[252:253], v[196:199]
  v_mfma_f32_4x4x4_16b_bf16 v[200:203], a[\d+4:\d+5], v[252:253], v[200:203]
  v_mfma_f32_4x4x4_16b_bf16 v[204:207], a[\d+6:\d+7], v[252:253], v[204:207]
  v_mfma_f32_4x4x4_16b_bf16 v[208:211], a[\d+8:\d+9], v[252:253], v[208:211]
  v_mfma_f32_4x4x4_16b_bf16 v[212:215], a[\d+10:\d+11], v[252:253], v[212:215]
.endm
.macro BAILCHK code
  s_add_u32 s70, s70, 1
  s_cmp_lt_u32 s70, SPINCAP
  s_cbranch_scc1 .Lnb\@
  v_mov_b32 v248, \code
  v_mov_b32 v249, 0
  buffer_store_dword v248, v249, s[44:47], 0 offen sc1
  s_waitcnt vmcnt(0)
  s_endpgm
.Lnb\@:
  s_sleep 2
.endm
.macro GUARD cnt
  v_mov_b32 v246, s78
  s_mov_b32 s70, 0
.Lg\@:
  ds_read_b32 v254, v246 offset:\cnt
  s_waitcnt lgkmcnt(0)
  v_readfirstlane_b32 s73, v254
  s_cmp_ge_u32 s73, 5
  s_cbranch_scc1 .Lh\@
  s_sleep 8
  s_add_u32 s70, s70, 1
  s_cmp_lt_u32 s70, SPINCAP
  s_cbranch_scc1 .Lg\@
  s_endpgm
.Lh\@:
.endm
.macro ARRIVE cnt
  v_mov_b32 v246, s78
  v_mov_b32 v254, 1
  s_waitcnt lgkmcnt(0)
  s_mov_b64 exec, 1
  ds_add_rtn_u32 v254, v246, v254 offset:\cnt
  s_mov_b64 exec, -1
  s_waitcnt lgkmcnt(0)
  v_readfirstlane_b32 s73, v254
  s_cmp_eq_u32 s73, 3
.endm
.macro DONE cnt
  v_mov_b32 v246, s78
  v_mov_b32 v254, 1
  s_mov_b64 exec, 1
  ds_add_u32 v246, v254 offset:\cnt
  s_mov_b64 exec, -1
.endm
.macro RED6 op, r
  s_nop 1
  \op v[\r], v[\r], v[\r] quad_perm:[1,0,3,2] row_mask:0xf bank_mask:0xf
  s_nop 1
  \op v[\r], v[\r], v[\r] quad_perm:[2,3,0,1] row_mask:0xf bank_mask:0xf
  s_nop 1
  \op v[\r], v[\r], v[\r] row_half_mirror row_mask:0xf bank_mask:0xf
  s_nop 1
  \op v[\r], v[\r], v[\r] row_mirror row_mask:0xf bank_mask:0xf
  s_nop 1
  \op v[\r], v[\r], v[\r] row_bcast:15 row_mask:0xa bank_mask:0xf
  s_nop 1
  \op v[\r], v[\r], v[\r] row_bcast:31 row_mask:0xc bank_mask:0xf
.endm
.macro XMSK r, j
  s_sub_i32 s73, s63, 8*\j
  v_cmp_gt_i32 vcc, s73, v244
  v_mul_f32 v[\r], s57, v[\r]
  s_nop 0
  v_cndmask_b32 v[\r], v255, v[\r], vcc
.endm
.macro XEXP r, so
  v_max_f32 v[\r], s73, v[\r]
  v_floor_f32 v253, v[\r]
  v_sub_f32 v[\r], v[\r], v253
  v_subrev_f32 v253, s72, v253
  v_exp_f32 v[\r], v[\r]
  v_cvt_i32_f32 v253, v253
  v_cvt_pk_bf16_f32 v[\r], v[\r], v[\r]
  v_lshlrev_b32 v[\r], 16, v[\r]
  v_ldexp_f32 v[\r], v[\r], v253
  v_cvt_pk_bf16_f32 v[\r], v[\r], v[\r]
  v_and_or_b32 v[\r], v[\r], v255, v252
  ds_write_b16 v219, v[\r] offset:\so
.endm
.macro XPUB so, stg, xo, second, dsg, isn, cnt, guard
  s_waitcnt lgkmcnt(0)
  ds_read_b32 v248, v239 offset:\so
  ds_read_b32 v249, v239 offset:\so+256
  ds_read_b32 v251, v239 offset:\so+512
.if \guard
  GUARD \cnt-4
.endif
  v_mov_b32 v255, 0xff800000
.if \isn
  s_mov_b32 s63, s56
.else
  s_mov_b32 s63, s89
.endif
  s_waitcnt lgkmcnt(0)
  XMSK 248, 0
  XMSK 249, 1
  XMSK 251, 2
  v_max3_f32 v253, v248, v249, v251
  RED6 v_max_f32_dpp, 253
  v_ceil_f32 v253, v253
  v_cvt_i32_f32 v253, v253
  s_mov_b32 s72, -500000
  s_mov_b32 s73, 500000
  v_max_i32 v253, s72, v253
  v_min_i32 v253, s73, v253
  v_cvt_f32_i32 v254, v253
  v_mov_b32 v255, 0x8000
  v_readlane_b32 s63, v253, 63
  v_readlane_b32 s72, v254, 63
.if \second
  v_cndmask_b32 v252, 0, v255, s[68:69]
.else
  v_cndmask_b32 v252, v255, 0, s[68:69]
.endif
  v_mov_b32 v255, 0x7fff
  s_mov_b32 s73, 0xca800000
  XEXP 248, \stg
  XEXP 249, \stg+128
  s_mov_b64 exec, 0xff
  XEXP 251, \stg+256
  s_add_u32 s63, s63, 0x100000
  s_lshr_b32 s73, s63, 15
  s_and_b32 s63, s63, 0x7fff
  s_lshl_b32 s73, s73, 16
  s_or_b32 s63, s63, s73
.if \second
  s_bitset1_b32 s63, 31
.else
  s_bitset1_b32 s63, 15
.endif
  s_mov_b64 exec, 3
  v_mov_b32 v248, s63
  v_mov_b32 v249, s63
  ds_write_b64 v220, v[248:249] offset:\stg+272
  s_mov_b64 exec, -1
  ARRIVE \cnt
  s_cbranch_scc0 .Lxp\@
  v_add_u32 v246, s78, v240
  s_cmp_eq_u32 s[\dsg], 0
  s_cbranch_scc1 .Lxq\@
  ds_read_b128 v[252:255], v246 offset:\stg
  s_add_u32 s62, s49, \xo
  s_waitcnt lgkmcnt(0)
  buffer_store_dwordx4 v[252:255], v240, s[44:47], s62 offen sc1
  s_mov_b64 exec, 0xff
  s_nop 0
  ds_read_b128 v[252:255], v246 offset:\stg+1024
  s_waitcnt lgkmcnt(0)
  buffer_store_dwordx4 v[252:255], v240, s[44:47], s62 offen offset:1024 sc1
  s_mov_b64 exec, -1
  s_nop 0
.Lxq\@:
  DONE \cnt
.Lxp\@:
.endm
.macro LEXP r, so
  v_max_f32 v[\r], s73, v[\r]
  v_floor_f32 v253, v[\r]
  v_sub_f32 v[\r], v[\r], v253
  v_subrev_f32 v253, s72, v253
  v_exp_f32 v[\r], v[\r]
  v_cvt_i32_f32 v253, v253
  v_cvt_pk_bf16_f32 v[\r], v[\r], v[\r]
  v_lshlrev_b32 v[\r], 16, v[\r]
  v_ldexp_f32 v[\r], v[\r], v253
  v_cvt_pk_bf16_f32 v[\r], v[\r], v[\r]
  ds_write_b16 v246, v[\r] offset:\so
  v_lshlrev_b32 v[\r], 16, v[\r]
.endm
.macro XLOC so
  s_waitcnt lgkmcnt(0)
  ds_read_b32 v248, v239 offset:\so
  ds_read_b32 v249, v239 offset:\so+256
  ds_read_b32 v251, v239 offset:\so+512
  v_mov_b32 v255, 0xff800000
  s_mov_b32 s63, s89
  v_lshrrev_b32 v246, 3, v240
  v_add_u32 v246, s74, v246
  s_waitcnt lgkmcnt(0)
  XMSK 248, 0
  XMSK 249, 1
  XMSK 251, 2
  v_max3_f32 v253, v248, v249, v251
  RED6 v_max_f32_dpp, 253
  v_ceil_f32 v253, v253
  v_cvt_i32_f32 v253, v253
  s_mov_b32 s72, -500000
  s_mov_b32 s73, 500000
  v_max_i32 v253, s72, v253
  v_min_i32 v253, s73, v253
  v_cvt_f32_i32 v254, v253
  s_mov_b32 s73, 0xca800000
  v_readlane_b32 s58, v253, 63
  v_readlane_b32 s72, v254, 63
  LEXP 248, PFC
  LEXP 249, PFC+128
  LEXP 251, PFC+256
  v_add_f32 v248, v248, v249
  v_add_f32 v248, v248, v251
  RED6 v_add_f32_dpp, 248
  s_nop 0
  v_readlane_b32 s59, v248, 63
  s_waitcnt lgkmcnt(0)
.endm
.macro XLOAD xo
  s_add_u32 s62, s48, \xo
  v_lshrrev_b32 v246, 1, v240
  s_mov_b32 exec_hi, 7
  buffer_load_dwordx2 v[216:217], v246, s[44:47], s62 offen sc1
  s_mov_b32 exec_hi, -1
.endm
.macro XLOADC xo, dsg
  s_cmp_eq_u32 s[\dsg], 0
  s_cbranch_scc1 .Lxl\@
  XLOAD \xo
  s_branch .Lxm\@
.Lxl\@:
  v_mov_b32 v246, -16
  buffer_load_dwordx2 v[216:217], v246, s[44:47], 0 offen
.Lxm\@:
.endm
.macro XUSE stg, xo, second, dsg, code
  ds_read_b64 v[248:249], v220 offset:\stg
  s_mov_b32 s72, s[\dsg]
  s_mov_b32 s70, 0
  s_cmp_eq_u32 s72, 0
  s_cbranch_scc1 .Lxz\@
.Lxc\@:
  v_and_b32 v254, v216, v217
.if \second
  v_cmp_gt_i32 vcc, 0, v254
.else
  v_lshlrev_b32 v254, 16, v254
  v_cmp_gt_i32 vcc, 0, v254
.endif
  s_or_b32 vcc_hi, vcc_hi, -8
  s_cmp_eq_u64 vcc, -1
  s_cbranch_scc1 .Lxo\@
  BAILCHK \code
  XLOAD \xo
  s_waitcnt vmcnt(0)
  s_branch .Lxc\@
.Lxz\@:
  s_waitcnt lgkmcnt(0)
  v_mov_b32 v216, v248
  v_mov_b32 v217, v249
.Lxo\@:
  s_waitcnt lgkmcnt(0)
  v_lshrrev_b32 v254, 5, v240
  v_readlane_b32 s63, v248, 34
  v_readlane_b32 s73, v216, 34
  v_lshrrev_b32 v254, v254, s72
  s_bfe_u32 s58, s63, 0xf0010
)ASM"
R"ASM(  s_and_b32 s63, s63, 0x7fff
  s_lshl_b32 s58, s58, 15
  s_or_b32 s63, s63, s58
  s_bfe_u32 s58, s73, 0xf0010
  s_and_b32 s73, s73, 0x7fff
  s_lshl_b32 s58, s58, 15
  s_or_b32 s73, s73, s58
  v_and_b32 v254, 1, v254
  s_max_i32 s58, s63, s73
  v_cmp_eq_u32 vcc, 1, v254
  s_sub_i32 s63, s63, s58
  s_sub_i32 s73, s73, s58
  v_mov_b32 v254, s63
  v_mov_b32 v255, s73
  s_mov_b32 s63, 0x7fff7fff
  v_cndmask_b32 v248, v248, v216, vcc
  v_cndmask_b32 v249, v249, v217, vcc
  v_cndmask_b32 v254, v254, v255, vcc
  v_and_b32 v248, s63, v248
  v_and_b32 v249, s63, v249
  v_lshlrev_b32 v252, 16, v248
  v_and_b32 v248, 0xffff0000, v248
  v_lshlrev_b32 v253, 16, v249
  v_and_b32 v249, 0xffff0000, v249
  v_ldexp_f32 v252, v252, v254
  v_ldexp_f32 v248, v248, v254
  v_ldexp_f32 v253, v253, v254
  v_ldexp_f32 v249, v249, v254
  v_cvt_pk_bf16_f32 v216, v252, v248
  v_cvt_pk_bf16_f32 v217, v253, v249
  v_lshrrev_b32 v246, 1, v240
  v_add_u32 v246, s74, v246
  v_add_f32 v252, v252, v248
  v_add_f32 v253, v253, v249
  ds_write_b64 v246, v[216:217] offset:PFC
  v_add_f32 v252, v252, v253
  v_cmp_gt_u32 vcc, 0x220, v240
  s_sub_i32 s58, s58, 0x100000
  s_nop 0
  v_cndmask_b32 v252, 0, v252, vcc
  RED6 v_add_f32_dpp, 252
  s_nop 0
  v_readlane_b32 s59, v252, 63
  s_waitcnt lgkmcnt(0)
.endm
.macro SW32 a, b
  v_permlane32_swap_b32 v[\a], v[\b]
.endm
.macro SW16 a, b
  v_permlane16_swap_b32 v[\a], v[\b]
.endm
.macro PUBP sreg, po, tag, rec, cnt, guard, second, stash
  s_nop 7
  SW32 192, 194
  SW32 193, 195
  SW32 196, 198
  SW32 197, 199
  SW32 200, 202
  SW32 201, 203
  SW32 204, 206
  SW32 205, 207
  SW32 208, 210
  SW32 209, 211
  SW32 212, 214
  SW32 213, 215
  s_nop 1
  v_add_f32 v192, v192, v194
  v_add_f32 v193, v193, v195
  v_add_f32 v196, v196, v198
  v_add_f32 v197, v197, v199
  v_add_f32 v200, v200, v202
  v_add_f32 v201, v201, v203
  v_add_f32 v204, v204, v206
  v_add_f32 v205, v205, v207
  v_add_f32 v208, v208, v210
  v_add_f32 v209, v209, v211
  v_add_f32 v212, v212, v214
  v_add_f32 v213, v213, v215
  s_nop 1
  SW16 192, 193
  SW16 196, 197
  SW16 200, 201
  SW16 204, 205
  SW16 208, 209
  SW16 212, 213
  s_nop 1
  v_add_f32 v192, v192, v193
  v_add_f32 v196, v196, v197
  v_add_f32 v200, v200, v201
  v_add_f32 v204, v204, v205
  v_add_f32 v208, v208, v209
  v_add_f32 v212, v212, v213
  v_mov_b32 v251, s58
  v_mov_b32 v252, s59
  v_add_f32_dpp v248, v192, v192 row_ror:8 row_mask:0xf bank_mask:0x3
  v_add_f32_dpp v249, v200, v200 row_ror:8 row_mask:0xf bank_mask:0x3
  v_add_f32_dpp v250, v208, v208 row_ror:8 row_mask:0xf bank_mask:0x3
  v_add_f32_dpp v248, v196, v196 row_ror:8 row_mask:0xf bank_mask:0xc
  v_add_f32_dpp v249, v204, v204 row_ror:8 row_mask:0xf bank_mask:0xc
  v_add_f32_dpp v250, v212, v212 row_ror:8 row_mask:0xf bank_mask:0xc
  v_cndmask_b32 v251, v251, v252, s[68:69]
.if \guard
  GUARD \cnt-4
.endif
  v_add_u32 v253, s74, v240
  ds_write_b128 v253, v[248:251] offset:\rec
  ARRIVE \cnt
  s_cbranch_scc0 .Lpp\@
  v_add_u32 v253, s79, v240
  v_add_u32 v255, s78, v240
  ds_read_b128 v[192:195], v253 offset:\rec+0*WMISC
  ds_read_b128 v[196:199], v253 offset:\rec+1*WMISC
  ds_read_b128 v[200:203], v253 offset:\rec+2*WMISC
  ds_read_b128 v[204:207], v253 offset:\rec+3*WMISC
.if \second
  ds_read_b128 v[208:211], v255 offset:\stash
.endif
  s_add_u32 s62, s[\sreg], \po
  s_waitcnt lgkmcnt(0)
  v_mov_b32_dpp v248, v195 quad_perm:[0,0,2,2] row_mask:0xf bank_mask:0xf
  v_mov_b32_dpp v249, v199 quad_perm:[0,0,2,2] row_mask:0xf bank_mask:0xf
  v_mov_b32_dpp v250, v203 quad_perm:[0,0,2,2] row_mask:0xf bank_mask:0xf
  v_mov_b32_dpp v251, v207 quad_perm:[0,0,2,2] row_mask:0xf bank_mask:0xf
.if \second
  v_mov_b32_dpp v252, v211 quad_perm:[0,0,2,2] row_mask:0xf bank_mask:0xf
  s_cmp_eq_u32 s88, 0
  s_cbranch_scc0 .Lps\@
  v_mov_b32 v252, v248
  v_mov_b32 v208, 0
  v_mov_b32 v209, 0
  v_mov_b32 v210, 0
  v_mov_b32 v211, 0
.Lps\@:
  v_max3_i32 v212, v248, v249, v252
.else
  v_max_i32 v212, v248, v249
.endif
  v_max3_i32 v212, v212, v250, v251
  v_sub_u32 v248, v248, v212
  v_sub_u32 v249, v249, v212
  v_sub_u32 v250, v250, v212
  v_sub_u32 v251, v251, v212
  v_ldexp_f32 v192, v192, v248
  v_ldexp_f32 v193, v193, v248
  v_ldexp_f32 v194, v194, v248
  v_ldexp_f32 v195, v195, v248
  v_ldexp_f32 v196, v196, v249
  v_ldexp_f32 v197, v197, v249
  v_ldexp_f32 v198, v198, v249
  v_ldexp_f32 v199, v199, v249
  v_ldexp_f32 v200, v200, v250
  v_ldexp_f32 v201, v201, v250
  v_ldexp_f32 v202, v202, v250
  v_ldexp_f32 v203, v203, v250
  v_ldexp_f32 v204, v204, v251
  v_ldexp_f32 v205, v205, v251
  v_ldexp_f32 v206, v206, v251
  v_ldexp_f32 v207, v207, v251
.if \second
  v_sub_u32 v252, v252, v212
  v_ldexp_f32 v208, v208, v252
  v_ldexp_f32 v209, v209, v252
  v_ldexp_f32 v210, v210, v252
  v_ldexp_f32 v211, v211, v252
  v_add_f32 v192, v192, v208
  v_add_f32 v193, v193, v209
  v_add_f32 v194, v194, v210
  v_add_f32 v195, v195, v211
.endif
  v_add_f32 v192, v192, v196
  v_add_f32 v200, v200, v204
  v_add_f32 v193, v193, v197
  v_add_f32 v201, v201, v205
  v_add_f32 v194, v194, v198
  v_add_f32 v202, v202, v206
  v_add_f32 v195, v195, v199
  v_add_f32 v203, v203, v207
  v_add_f32 v248, v192, v200
  v_add_f32 v249, v193, v201
  v_add_f32 v250, v194, v202
  v_add_f32 v251, v195, v203
.if \second == 0
  s_cmp_eq_u32 s88, 0
  s_cbranch_scc1 .Lpq\@
  v_cndmask_b32 v251, v212, v251, s[68:69]
  ds_write_b128 v255, v[248:251] offset:\stash
  s_waitcnt lgkmcnt(0)
  s_branch .Lpr\@
.Lpq\@:
.endif
  v_cvt_f32_i32 v212, v212
  v_cndmask_b32 v251, v212, v251, s[68:69]
  v_and_or_b32 v248, v248, -16, \tag
  v_and_or_b32 v249, v249, -16, \tag
  v_and_or_b32 v250, v250, -16, \tag
  v_and_or_b32 v251, v251, -16, \tag
  buffer_store_dwordx4 v[248:251], v240, s[44:47], s62 offen sc1
  s_nop 1
.Lpr\@:
  DONE \cnt
.Lpp\@:
.endm
.macro QLOAD sreg, qo, r
  s_add_u32 s62, s[\sreg], \qo
  s_mov_b32 exec_hi, 0xffff
  buffer_load_dwordx4 v[\r:\r+3], v240, s[44:47], s62 offen sc1
  s_mov_b32 exec_hi, -1
.endm
.macro QUSE sreg, qo, r, q, code, tdesc=0, t0=0, t1=0, ta=0, tb=0
  s_mov_b32 s70, 0
.if \tdesc
  s_mov_b32 s63, \t0
.endif
.Lqc\@:
  v_and_b32 v248, v[\r], v[\r+1]
  v_and_b32 v249, v[\r+2], v[\r+3]
  v_and_b32 v248, v248, v249
  v_and_b32 v248, 15, v248
  v_cmp_eq_u32 vcc, 1, v248
  s_or_b32 vcc_hi, vcc_hi, 0xffff0000
  s_cmp_eq_u64 vcc, -1
  s_cbranch_scc1 .Lqo\@
  BAILCHK \code
.if \tdesc
  s_cmp_ge_u32 s63, \t1
  s_cbranch_scc1 .Lqt\@
  LDV \tdesc, s63, 36, \ta,
  s_add_u32 s63, s63, 1
  LDV \tdesc, s63, 36, \tb,
  s_add_u32 s63, s63, 1
.Lqt\@:
.endif
  QLOAD \sreg, \qo, \r
  s_waitcnt vmcnt(0)
  s_branch .Lqc\@
.Lqo\@:
  v_perm_b32 v[\r], v[\r+1], v[\r], s71
  v_perm_b32 v[\r+1], v[\r+3], v[\r+2], s71
  v_lshrrev_b32 v254, 1, v240
  v_add_u32 v254, s74, v254
  s_nop 0
  ds_write_b64 v254, v[\r:\r+1] offset:QSTAGEC
  s_waitcnt lgkmcnt(0)
  ds_read_b128 v[\q:\q+3], v245
  ds_read_b128 v[\q+4:\q+7], v245 offset:128
  ds_read_b128 v[\q+8:\q+11], v245 offset:256
  s_waitcnt lgkmcnt(0)
.endm
.macro DLOAD r, off
  v_lshrrev_b32 v254, 2, v240
  v_add_u32 v254, s55, v254
  s_nop 0
  ds_read_b32 v[\r], v254 offset:\off
  s_waitcnt lgkmcnt(0)
.endm
  s_branch .Lstream_body
.subsection 1
.p2align 8
.Lstream_body:
  DLOAD 233, 0
  v_readlane_b32 s80, v233, 40
  v_readlane_b32 s84, v233, 41
  DLOAD 233, 512
  v_readlane_b32 s81, v233, 40
  v_readlane_b32 s85, v233, 41
  DLOAD 233, 1024
  v_readlane_b32 s82, v233, 40
  v_readlane_b32 s86, v233, 41
  DLOAD 233, 1536
  v_readlane_b32 s83, v233, 40
  v_readlane_b32 s87, v233, 41
  DLOAD 233, 0
  LDV 233, 0, 36, 48, nt
  LDL 233, 1, 36, 0, nt
  LDV 233, 2, 36, 60, nt
  LDL 233, 3, 36, 3072, nt
  s_waitcnt vmcnt(9)
  LDV 233, 4, 36, 72, nt
  s_waitcnt vmcnt(9)
  LDL 233, 5, 36, 6144, nt
  s_waitcnt vmcnt(9)
  LDV 233, 6, 36, 84, nt
  s_waitcnt vmcnt(9)
  LDL 233, 7, 36, 9216, nt
  s_waitcnt vmcnt(9)
  LDV 233, 8, 36, 96, nt
  s_waitcnt vmcnt(9)
  LDL 233, 9, 36, 12288, nt
  s_waitcnt vmcnt(9)
  LDV 233, 10, 36, 108, nt
  s_waitcnt vmcnt(9)
  LDL 233, 11, 36, 15360, nt
  s_waitcnt vmcnt(9)
  LDV 233, 12, 36, 120, nt
  s_waitcnt vmcnt(9)
  LDL 233, 13, 36, 18432, nt
  s_waitcnt vmcnt(9)
  LDV 233, 14, 36, 132, nt
  s_waitcnt vmcnt(9)
  LDL 233, 15, 36, 21504, nt
  s_waitcnt vmcnt(9)
  LDV 233, 16, 36, 144, nt
  s_waitcnt vmcnt(9)
  LDL 233, 17, 36, 24576, nt
  s_waitcnt vmcnt(9)
  LDV 233, 18, 36, 156, nt
  s_waitcnt vmcnt(9)
  LDL 233, 19, 36, 27648, nt
  s_waitcnt vmcnt(9)
  LDL 233, 20, 36, 30720, nt
  s_waitcnt vmcnt(9)
  DLOAD 234, 256
  LDA 234, 0, 40, 0, nt
  s_waitcnt vmcnt(9)
  LDA 234, 1, 40, 12, nt
  s_waitcnt vmcnt(9)
  LDA 234, 2, 40, 24, nt
  s_waitcnt vmcnt(9)
  LDA 234, 3, 40, 36, nt
  s_waitcnt vmcnt(9)
  LDA 234, 4, 40, 48, nt
  s_waitcnt vmcnt(9)
  LDA 234, 5, 40, 60, nt
  s_waitcnt vmcnt(9)
  LDA 234, 6, 40, 72, nt
  s_waitcnt vmcnt(9)
  LDA 234, 7, 40, 84, nt
  s_waitcnt vmcnt(9)
  LDA 234, 8, 40, 96, nt
  s_waitcnt vmcnt(9)
  LDA 234, 9, 40, 108, nt
  s_waitcnt vmcnt(9)
  LDA 234, 10, 40, 120, nt
  s_waitcnt vmcnt(9)
  LDA 234, 11, 40, 132, nt
  s_waitcnt vmcnt(9)
  LDA 234, 12, 40, 144, nt
  s_waitcnt vmcnt(9)
  LDA 234, 13, 40, 156, nt
  s_waitcnt vmcnt(9)
  LDV 233, 21, 36, 0, 
  QLOAD 52, 0, 222
  QLOAD 53, 0, 226
  s_waitcnt vmcnt(11)
  LDV 233, 22, 36, 12, 
  s_waitcnt vmcnt(11)
  s_waitcnt vmcnt(8)
  s_waitcnt vmcnt(4)
  QUSE 52, 0, 222, 168, 1
  s_waitcnt vmcnt(3)
  QUSE 53, 0, 226, 180, 2
  L2V 24, 0
  s_waitcnt lgkmcnt(0)
  KC2 48, 24, 168, 0
  LDV 233, 23, 36, 36, 
  KC 0, 180, 672
  L2V 24, 3072
  s_waitcnt lgkmcnt(0)
  L2V 0, 6144
  KC2 60, 24, 168, 64
  LDV 233, 24, 36, 24, 
  s_waitcnt lgkmcnt(0)
  KC2 72, 0, 168, 128
  s_waitcnt vmcnt(6)
  KC 12, 180, 704
  L2V 0, 9216
  s_waitcnt lgkmcnt(0)
  L2V 12, 12288
  KC2 84, 0, 168, 192
  LDV 233, 25, 36, 0, 
  s_waitcnt lgkmcnt(0)
  KC2 96, 12, 168, 256
  s_waitcnt vmcnt(6)
  KC 36, 180, 736
  L2V 12, 15360
  s_waitcnt lgkmcnt(0)
  L2V 36, 18432
  KC2 108, 12, 168, 320
  LDV 233, 26, 36, 12, 
  s_waitcnt lgkmcnt(0)
  KC2 120, 36, 168, 384
  s_waitcnt vmcnt(6)
  KC 24, 180, 768
  L2V 36, 21504
  s_waitcnt lgkmcnt(0)
  L2V 24, 24576
  KC2 132, 36, 168, 448
  LDV 233, 27, 36, 36, 
  s_waitcnt lgkmcnt(0)
  KC2 144, 24, 168, 512
  s_waitcnt vmcnt(6)
  KC 0, 180, 800
  L2V 24, 27648
  s_waitcnt lgkmcnt(0)
  KC2 156, 24, 168, 576
  LDV 233, 28, 36, 0, 
  L2V 24, 30720
  s_waitcnt lgkmcnt(0)
  KC 24, 168, 640
  LDV 233, 29, 36, 24, 
)ASM"
R"ASM(  s_waitcnt vmcnt(9)
  KC 12, 180, 832
  LDV 233, 30, 36, 12, 
  s_waitcnt vmcnt(9)
  KC 36, 180, 864
  LDV 233, 31, 36, 36, 
  s_waitcnt vmcnt(9)
  KC 0, 180, 896
  LDV 233, 32, 36, 0, 
  s_waitcnt vmcnt(9)
  KC 24, 180, 928
  LDV 233, 33, 36, 24, 
  s_waitcnt vmcnt(9)
  KC 12, 180, 960
  LDV 233, 34, 36, 12, 
  s_waitcnt vmcnt(9)
  KC 36, 180, 992
  LDV 233, 35, 36, 36, 
  s_waitcnt vmcnt(9)
  KC 0, 180, 1024
  LDV 233, 36, 36, 0, 
  s_waitcnt vmcnt(9)
  KC 24, 180, 1056
  LDV 233, 37, 36, 24, 
  s_waitcnt vmcnt(9)
  KC 12, 180, 1088
  LDA 234, 14, 40, 168, nt
  s_waitcnt vmcnt(9)
  KC 36, 180, 1120
  LDA 234, 15, 40, 180, nt
  s_waitcnt vmcnt(9)
  KC 0, 180, 1152
  LDA 234, 16, 40, 192, nt
  s_waitcnt vmcnt(9)
  KC 24, 180, 1184
  LDA 234, 17, 40, 204, nt
  XPUB 672, 0, 0, 0, 84, 1, 2320, 0
  XLOC 0
  PPRD 1792
  VPRE
  PPRD 1808
  s_nop 0
  VC0A 0
  VPRE
  PPRD 1824
  s_nop 0
  VCA 12
  VPRE
  PPRD 1840
  s_nop 0
  VCA 24
  LDA 234, 18, 40, 216, nt
  VPRE
  PPRD 1856
  s_nop 0
  VCA 36
  VPRE
  PPRD 1872
  s_nop 0
  VCA 48
  VPRE
  PPRD 1888
  s_nop 0
  VCA 60
  LDA 234, 19, 40, 228, nt
  VPRE
  PPRD 1904
  s_nop 0
  VCA 72
  VPRE
  PPRD 1920
  s_nop 0
  VCA 84
  VPRE
  PPRD 1936
  s_nop 0
  VCA 96
  VPRE
  PPRD 1952
  s_nop 0
  VCA 108
  LDA 234, 20, 40, 240, nt
  VPRE
  PPRD 1968
  s_nop 0
  VCA 120
  VPRE
  PPRD 1984
  s_nop 0
  VCA 132
  VPRE
  PPRD 2000
  s_nop 0
  VCA 144
  LDV 234, 21, 40, 12, 
  VPRE
  PPRD 2016
  s_nop 0
  VCA 156
  s_waitcnt vmcnt(21)
  VPRE
  PPRD 2032
  s_nop 0
  VCA 168
  s_waitcnt vmcnt(18)
  VPRE
  PPRD 2048
  s_nop 0
  VCA 180
  s_waitcnt vmcnt(15)
  VPRE
  PPRD 2064
  s_nop 0
  VCA 192
  s_waitcnt vmcnt(12)
  VPRE
  s_nop 1
  VCA 204
  XLOADC 0, 84
  PPRD 2080
  s_waitcnt vmcnt(10)
  VPRE
  PPRD 2096
  s_nop 0
  VCA 216
  LDV 234, 22, 40, 36, 
  s_waitcnt vmcnt(10)
  VPRE
  PPRD 2112
  s_nop 0
  VCA 228
  LDV 234, 23, 40, 0, 
  s_waitcnt vmcnt(10)
  VPRE
  s_nop 1
  VCA 240
  LDV 234, 24, 40, 24, 
  PUBP 50, 0, 1, 2816, 2368, 0, 0, 2560
  s_waitcnt vmcnt(9)
  XUSE 0, 0, 0, 84, 3
  PPRD 1792
  VPRE
  PPRD 1808
  s_nop 0
  VC0V 12
  LDV 234, 25, 40, 12, 
  s_waitcnt vmcnt(9)
  VPRE
  PPRD 1824
  s_nop 0
  VCV 36
  LDV 234, 26, 40, 36, 
  s_waitcnt vmcnt(9)
  VPRE
  PPRD 1840
  s_nop 0
  VCV 0
  LDV 234, 27, 40, 0, 
  s_waitcnt vmcnt(9)
  VPRE
  PPRD 1856
  s_nop 0
  VCV 24
  LDV 234, 28, 40, 24, 
  s_waitcnt vmcnt(9)
  VPRE
  PPRD 1872
  s_nop 0
  VCV 12
  LDV 234, 29, 40, 12, 
  s_waitcnt vmcnt(9)
  VPRE
  PPRD 1888
  s_nop 0
  VCV 36
  LDV 234, 30, 40, 36, 
  s_waitcnt vmcnt(9)
  VPRE
  PPRD 1904
  s_nop 0
  VCV 0
  LDV 234, 31, 40, 0, 
  s_waitcnt vmcnt(9)
  VPRE
  PPRD 1920
  s_nop 0
  VCV 24
  LDV 234, 32, 40, 24, 
  s_waitcnt vmcnt(9)
  VPRE
  PPRD 1936
  s_nop 0
  VCV 12
  LDV 234, 33, 40, 12, 
  s_waitcnt vmcnt(9)
  VPRE
  PPRD 1952
  s_nop 0
  VCV 36
  LDV 234, 34, 40, 36, 
  s_waitcnt vmcnt(9)
  VPRE
  PPRD 1968
  s_nop 0
  VCV 0
  LDV 234, 35, 40, 0, 
  s_waitcnt vmcnt(9)
  VPRE
  PPRD 1984
  s_nop 0
  VCV 24
  LDV 234, 36, 40, 24, 
  s_waitcnt vmcnt(9)
  VPRE
  PPRD 2000
  s_nop 0
  VCV 12
  LDV 234, 37, 40, 12, 
  s_waitcnt vmcnt(9)
  VPRE
  s_nop 1
  VCV 36
  QLOAD 53, 32768, 222
  PPRD 2016
  s_waitcnt vmcnt(7)
  VPRE
  PPRD 2032
  s_nop 0
  VCV 0
  s_waitcnt vmcnt(4)
  VPRE
  PPRD 2048
  s_nop 0
  VCV 24
  s_waitcnt vmcnt(1)
  VPRE
  s_nop 1
  VCV 12
  DLOAD 235, 512
  LDV 235, 21, 36, 36, 
  PUBP 51, 0, 1, 2816, 2372, 1, 1, 2560
  s_waitcnt vmcnt(3)
  QUSE 53, 32768, 222, 168, 4
  s_waitcnt vmcnt(0)
  KC 36, 168, 672
  LDV 235, 22, 36, 0, 
  s_waitcnt vmcnt(0)
  KC 0, 168, 704
  LDV 235, 23, 36, 24, 
  s_waitcnt vmcnt(0)
  KC 24, 168, 736
  LDV 235, 24, 36, 12, 
  s_waitcnt vmcnt(0)
  KC 12, 168, 768
  LDV 235, 25, 36, 36, 
  s_waitcnt vmcnt(0)
  KC 36, 168, 800
  LDV 235, 26, 36, 0, 
  s_waitcnt vmcnt(0)
  KC 0, 168, 832
  LDV 235, 27, 36, 24, 
  s_waitcnt vmcnt(0)
  KC 24, 168, 864
  LDV 235, 28, 36, 12, 
  LDV 235, 29, 36, 36, 
  LDV 235, 30, 36, 0, 
  s_waitcnt vmcnt(6)
  KC 12, 168, 896
  LDV 235, 31, 36, 24, 
  s_waitcnt vmcnt(6)
  KC 36, 168, 928
  LDV 235, 32, 36, 12, 
  s_waitcnt vmcnt(6)
  KC 0, 168, 960
  LDV 235, 33, 36, 36, 
  s_waitcnt vmcnt(6)
  KC 24, 168, 992
  LDV 235, 34, 36, 0, 
  s_waitcnt vmcnt(6)
  KC 12, 168, 1024
  LDV 235, 35, 36, 24, 
  s_waitcnt vmcnt(6)
  KC 36, 168, 1056
  LDV 235, 36, 36, 12, 
  s_waitcnt vmcnt(6)
  KC 0, 168, 1088
  LDV 235, 37, 36, 36, 
  s_waitcnt vmcnt(6)
  KC 24, 168, 1120
  DLOAD 236, 768
  LDV 236, 21, 40, 0, 
  s_waitcnt vmcnt(6)
  KC 12, 168, 1152
  LDV 236, 22, 40, 24, 
  s_waitcnt vmcnt(6)
  KC 36, 168, 1184
  LDV 236, 23, 40, 12, 
  XPUB 672, 0, 1152, 0, 85, 1, 2324, 1
  XLOADC 1152, 85
  s_waitcnt vmcnt(0)
  XUSE 0, 1152, 0, 85, 5
  LDV 236, 24, 40, 36, 
  PPRD 1792
  VPRE
  PPRD 1808
  s_nop 0
  VC0V 0
  LDV 236, 25, 40, 0, 
  VPRE
  PPRD 1824
  s_nop 0
  VCV 24
  LDV 236, 26, 40, 24, 
  VPRE
  PPRD 1840
  s_nop 0
  VCV 12
  s_waitcnt vmcnt(6)
  VPRE
  PPRD 1856
  s_nop 0
  VCV 36
  LDV 236, 27, 40, 12, 
  s_waitcnt vmcnt(6)
  VPRE
  PPRD 1872
  s_nop 0
  VCV 0
  LDV 236, 28, 40, 36, 
  s_waitcnt vmcnt(6)
  VPRE
  PPRD 1888
  s_nop 0
  VCV 24
  LDV 236, 29, 40, 0, 
  s_waitcnt vmcnt(6)
  VPRE
  PPRD 1904
  s_nop 0
  VCV 12
  LDV 236, 30, 40, 24, 
  s_waitcnt vmcnt(6)
  VPRE
  PPRD 1920
  s_nop 0
  VCV 36
  LDV 236, 31, 40, 12, 
  s_waitcnt vmcnt(6)
  VPRE
  PPRD 1936
  s_nop 0
  VCV 0
  LDV 236, 32, 40, 36, 
  s_waitcnt vmcnt(6)
  VPRE
  PPRD 1952
  s_nop 0
  VCV 24
  LDV 236, 33, 40, 0, 
  s_waitcnt vmcnt(6)
  VPRE
  PPRD 1968
  s_nop 0
  VCV 12
  LDV 236, 34, 40, 24, 
  s_waitcnt vmcnt(6)
  VPRE
  PPRD 1984
  s_nop 0
  VCV 36
  LDV 236, 35, 40, 12, 
  s_waitcnt vmcnt(6)
  VPRE
  PPRD 2000
  s_nop 0
  VCV 0
  LDV 236, 36, 40, 36, 
  s_waitcnt vmcnt(6)
  VPRE
  PPRD 2016
  s_nop 0
  VCV 24
  LDV 236, 37, 40, 0, 
  LDV 233, 21, 36, 24, nt
  s_waitcnt vmcnt(9)
  VPRE
  PPRD 2032
  s_nop 0
  VCV 12
  LDV 233, 22, 36, 12, nt
  s_waitcnt vmcnt(9)
  VPRE
  s_nop 1
  VCV 36
  QLOAD 52, 131072, 222
  PPRD 2048
  s_waitcnt vmcnt(7)
  VPRE
  s_nop 1
  VCV 0
)ASM"
R"ASM(  PUBP 51, 262144, 2, 2816, 2376, 1, 0, 2560
  s_waitcnt vmcnt(0)
  QUSE 52, 131072, 222, 168, 6
  QLOAD 53, 131072, 226
  QLOAD 52, 32768, 222
  L2V 36, 0
  s_waitcnt lgkmcnt(0)
  L2V 0, 3072
  KC2 48, 36, 168, 0
  LDV 235, 0, 36, 48, nt
  LDL 235, 1, 36, 0, nt
  s_waitcnt lgkmcnt(0)
  L2V 36, 6144
  KC2 60, 0, 168, 64
  LDV 235, 2, 36, 60, nt
  LDL 235, 3, 36, 3072, nt
  s_waitcnt lgkmcnt(0)
  L2V 0, 9216
  KC2 72, 36, 168, 128
  LDV 235, 4, 36, 72, nt
  LDL 235, 5, 36, 6144, nt
  s_waitcnt lgkmcnt(0)
  L2V 36, 12288
  KC2 84, 0, 168, 192
  LDV 235, 6, 36, 84, nt
  s_waitcnt lgkmcnt(0)
  L2V 0, 15360
  KC2 96, 36, 168, 256
  s_waitcnt lgkmcnt(0)
  L2V 36, 18432
  KC2 108, 0, 168, 320
  s_waitcnt lgkmcnt(0)
  L2V 0, 21504
  KC2 120, 36, 168, 384
  s_waitcnt lgkmcnt(0)
  L2V 36, 24576
  KC2 132, 0, 168, 448
  s_waitcnt lgkmcnt(0)
  L2V 0, 27648
  KC2 144, 36, 168, 512
  LDV 233, 23, 36, 36, nt
  LDL 235, 7, 36, 9216, nt
  s_waitcnt lgkmcnt(0)
  KC2 156, 0, 168, 576
  L2V 0, 30720
  s_waitcnt lgkmcnt(0)
  KC 0, 168, 640
  LDV 233, 24, 36, 0, nt
  s_waitcnt vmcnt(31)
  QUSE 53, 131072, 226, 180, 7
  s_waitcnt vmcnt(30)
  QUSE 52, 32768, 222, 168, 8
  XLOC 0
  PPRD 1792
  VPRE
  PPRD 1808
  s_nop 0
  VC0A 0
  VPRE
  PPRD 1824
  s_nop 0
  VCA 12
  VPRE
  PPRD 1840
  s_nop 0
  VCA 24
  VPRE
  PPRD 1856
  s_nop 0
  VCA 36
  VPRE
  PPRD 1872
  s_nop 0
  VCA 48
  VPRE
  PPRD 1888
  s_nop 0
  VCA 60
  VPRE
  PPRD 1904
  s_nop 0
  VCA 72
  VPRE
  PPRD 1920
  s_nop 0
  VCA 84
  VPRE
  PPRD 1936
  s_nop 0
  VCA 96
  VPRE
  PPRD 1952
  s_nop 0
  VCA 108
  VPRE
  PPRD 1968
  s_nop 0
  VCA 120
  VPRE
  PPRD 1984
  s_nop 0
  VCA 132
  VPRE
  PPRD 2000
  s_nop 0
  VCA 144
  VPRE
  PPRD 2016
  s_nop 0
  VCA 156
  VPRE
  PPRD 2032
  s_nop 0
  VCA 168
  VPRE
  PPRD 2048
  s_nop 0
  VCA 180
  VPRE
  PPRD 2064
  s_nop 0
  VCA 192
  VPRE
  PPRD 2080
  s_nop 0
  VCA 204
  VPRE
  PPRD 2096
  s_nop 0
  VCA 216
  VPRE
  PPRD 2112
  s_nop 0
  VCA 228
  VPRE
  s_nop 1
  VCA 240
  PUBP 50, 524288, 1, 2816, 2380, 1, 0, 3584
  KC 24, 180, 1216
  KC 12, 180, 1248
  s_waitcnt vmcnt(27)
  KC 48, 168, 0
  LDV 235, 8, 36, 96, nt
  s_waitcnt vmcnt(27)
  L2V 24, 0
  s_waitcnt lgkmcnt(0)
  KC 24, 168, 32
  LDL 235, 9, 36, 12288, nt
  s_waitcnt vmcnt(27)
  KC 60, 168, 64
  LDV 233, 25, 36, 12, nt
  s_waitcnt vmcnt(27)
  L2V 24, 3072
  s_waitcnt lgkmcnt(0)
  KC 24, 168, 96
  LDV 235, 10, 36, 108, nt
  s_waitcnt vmcnt(27)
  KC 72, 168, 128
  LDL 235, 11, 36, 15360, nt
  s_waitcnt vmcnt(27)
  L2V 24, 6144
  s_waitcnt lgkmcnt(0)
  KC 24, 168, 160
  LDV 233, 26, 36, 24, nt
  s_waitcnt vmcnt(27)
  KC 84, 168, 192
  LDV 235, 12, 36, 120, nt
  s_waitcnt vmcnt(27)
  KC 36, 180, 1280
  LDL 235, 13, 36, 18432, nt
  s_waitcnt vmcnt(27)
  L2V 36, 9216
  s_waitcnt lgkmcnt(0)
  KC 36, 168, 224
  LDV 233, 27, 36, 36, nt
  s_waitcnt vmcnt(27)
  KC 0, 180, 1312
  LDV 235, 14, 36, 132, nt
  s_waitcnt vmcnt(27)
  KC 96, 168, 256
  LDL 235, 15, 36, 21504, nt
  s_waitcnt vmcnt(27)
  L2V 0, 12288
  s_waitcnt lgkmcnt(0)
  KC 0, 168, 288
  LDV 233, 28, 36, 0, nt
  s_waitcnt vmcnt(27)
  KC 12, 180, 1344
  s_waitcnt vmcnt(24)
  KC 108, 168, 320
  s_waitcnt vmcnt(21)
  L2V 12, 15360
  s_waitcnt lgkmcnt(0)
  KC 12, 168, 352
  s_waitcnt vmcnt(18)
  KC 24, 180, 1376
  s_waitcnt vmcnt(15)
  KC 120, 168, 384
  s_waitcnt vmcnt(12)
  L2V 12, 18432
  s_waitcnt lgkmcnt(0)
  KC 12, 168, 416
  s_waitcnt vmcnt(9)
  KC 36, 180, 1408
  LDV 235, 16, 36, 144, nt
  s_waitcnt vmcnt(9)
  KC 132, 168, 448
  LDL 235, 17, 36, 24576, nt
  s_waitcnt vmcnt(9)
  L2V 24, 21504
  s_waitcnt lgkmcnt(0)
  KC 24, 168, 480
  LDV 233, 29, 36, 12, nt
  s_waitcnt vmcnt(9)
  KC 0, 180, 1440
  LDV 235, 18, 36, 156, nt
  s_waitcnt vmcnt(9)
  KC 144, 168, 512
  LDL 235, 19, 36, 27648, nt
  s_waitcnt vmcnt(9)
  L2V 36, 24576
  s_waitcnt lgkmcnt(0)
  KC 36, 168, 544
  LDV 233, 30, 36, 24, nt
  s_waitcnt vmcnt(9)
  KC 12, 180, 1472
  LDL 235, 20, 36, 30720, nt
  s_waitcnt vmcnt(9)
  KC 156, 168, 576
  LDA 236, 0, 40, 0, nt
  s_waitcnt vmcnt(9)
  L2V 0, 27648
  s_waitcnt lgkmcnt(0)
  KC 0, 168, 608
  LDA 236, 1, 40, 12, nt
  s_waitcnt vmcnt(9)
  KC 24, 180, 1504
  LDV 233, 31, 36, 36, nt
  s_waitcnt vmcnt(9)
  L2V 12, 30720
  s_waitcnt lgkmcnt(0)
  KC 12, 168, 640
  LDA 236, 2, 40, 24, nt
  s_waitcnt vmcnt(9)
  LDV 233, 32, 36, 0, nt
  XLOC 0
  PPRD 1792
  VPRE
  PPRD 1808
  s_nop 0
  VC0A 0
  s_waitcnt vmcnt(9)
  VPRE
  s_nop 1
  VCA 12
  LDA 236, 3, 40, 36, nt
  s_waitcnt vmcnt(9)
  KC 36, 180, 1536
  LDA 236, 4, 40, 48, nt
  PPRD 1824
  s_waitcnt vmcnt(9)
  VPRE
  s_nop 1
  VCA 24
  LDV 233, 33, 36, 24, nt
  s_waitcnt vmcnt(9)
  KC 0, 180, 1568
  LDA 236, 5, 40, 60, nt
  PPRD 1840
  s_waitcnt vmcnt(9)
  VPRE
  PPRD 1856
  s_nop 0
  VCA 36
  LDA 236, 6, 40, 72, nt
  s_waitcnt vmcnt(9)
  VPRE
  s_nop 1
  VCA 48
  LDV 233, 34, 36, 12, nt
  s_waitcnt vmcnt(9)
  KC 24, 180, 1600
  LDA 236, 7, 40, 84, nt
  PPRD 1872
  s_waitcnt vmcnt(9)
  VPRE
  PPRD 1888
  s_nop 0
  VCA 60
  LDA 236, 8, 40, 96, nt
  s_waitcnt vmcnt(9)
  VPRE
  s_nop 1
  VCA 72
  LDV 233, 35, 36, 36, nt
  s_waitcnt vmcnt(9)
  KC 12, 180, 1632
  LDA 236, 9, 40, 108, nt
  PPRD 1904
  s_waitcnt vmcnt(9)
  VPRE
  PPRD 1920
  s_nop 0
  VCA 84
  LDV 233, 36, 36, 0, nt
  s_waitcnt vmcnt(9)
  VPRE
  s_nop 1
  VCA 96
  LDA 236, 10, 40, 120, nt
  s_waitcnt vmcnt(9)
  KC 36, 180, 1664
  LDA 236, 11, 40, 132, nt
  PPRD 1936
  s_waitcnt vmcnt(9)
  VPRE
  s_nop 1
  VCA 108
  LDV 233, 37, 36, 24, nt
  s_waitcnt vmcnt(9)
  KC 0, 180, 1696
  LDA 236, 12, 40, 144, nt
  PPRD 1952
  s_waitcnt vmcnt(9)
  VPRE
  PPRD 1968
  s_nop 0
  VCA 120
  LDA 236, 13, 40, 156, nt
  s_waitcnt vmcnt(9)
  VPRE
  s_nop 1
  VCA 132
  LDA 236, 14, 40, 168, nt
  s_waitcnt vmcnt(9)
  KC 24, 180, 1728
  LDA 236, 15, 40, 180, nt
  PPRD 1984
  s_waitcnt vmcnt(9)
  VPRE
  PPRD 2000
  s_nop 0
  VCA 144
  LDA 236, 16, 40, 192, nt
  s_waitcnt vmcnt(9)
  VPRE
  s_nop 1
  VCA 156
  LDA 236, 17, 40, 204, nt
  XPUB 1216, 1152, 2304, 0, 84, 1, 2352, 0
  PPRD 2016
  s_waitcnt vmcnt(9)
  VPRE
  PPRD 2032
  s_nop 0
  VCA 168
  LDA 236, 18, 40, 216, nt
  s_waitcnt vmcnt(9)
  VPRE
  PPRD 2048
  s_nop 0
  VCA 180
  LDA 236, 19, 40, 228, nt
  s_waitcnt vmcnt(9)
  VPRE
  PPRD 2064
  s_nop 0
  VCA 192
  LDA 236, 20, 40, 240, nt
  s_waitcnt vmcnt(9)
  VPRE
  PPRD 2080
  s_nop 0
  VCA 204
  s_waitcnt vmcnt(6)
  VPRE
  PPRD 2096
  s_nop 0
  VCA 216
  DLOAD 237, 1024
  LDV 237, 21, 36, 12, 
  s_waitcnt vmcnt(6)
  VPRE
  s_nop 1
  VCA 228
  LDV 234, 21, 40, 36, nt
  XLOADC 2304, 84
  QLOAD 53, 65536, 222
  PPRD 2112
  s_waitcnt vmcnt(8)
  VPRE
  s_nop 1
  VCA 240
  LDV 237, 22, 36, 0, 
  PUBP 50, 262144, 2, 2816, 2384, 1, 1, 2560
  s_waitcnt vmcnt(3)
  QUSE 53, 65536, 222, 168, 9
  LDV 234, 22, 40, 24, nt
  XUSE 1152, 2304, 0, 84, 10
  KC 12, 168, 672
)ASM"
R"ASM(  LDV 237, 23, 36, 12, 
  PPRD 1792
  VPRE
  s_nop 1
  VC0V 36
  s_waitcnt vmcnt(6)
  KC 0, 168, 704
  LDV 237, 24, 36, 36, 
  PPRD 1808
  s_waitcnt vmcnt(6)
  VPRE
  s_nop 1
  VCV 24
  LDV 234, 23, 40, 0, nt
  s_waitcnt vmcnt(6)
  KC 12, 168, 736
  LDV 237, 25, 36, 24, 
  s_waitcnt vmcnt(6)
  KC 36, 168, 768
  LDV 234, 24, 40, 12, nt
  PPRD 1824
  s_waitcnt vmcnt(6)
  VPRE
  s_nop 1
  VCV 0
  LDV 237, 26, 36, 36, 
  s_waitcnt vmcnt(6)
  KC 24, 168, 800
  LDV 237, 27, 36, 0, 
  PPRD 1840
  s_waitcnt vmcnt(6)
  VPRE
  s_nop 1
  VCV 12
  LDV 234, 25, 40, 24, nt
  s_waitcnt vmcnt(6)
  KC 36, 168, 832
  LDV 237, 28, 36, 12, 
  s_waitcnt vmcnt(6)
  KC 0, 168, 864
  LDV 234, 26, 40, 36, nt
  PPRD 1856
  s_waitcnt vmcnt(6)
  VPRE
  s_nop 1
  VCV 24
  LDV 237, 29, 36, 0, 
  s_waitcnt vmcnt(6)
  KC 12, 168, 896
  LDV 237, 30, 36, 24, 
  PPRD 1872
  s_waitcnt vmcnt(6)
  VPRE
  s_nop 1
  VCV 36
  LDV 234, 27, 40, 12, nt
  s_waitcnt vmcnt(6)
  KC 0, 168, 928
  LDV 237, 31, 36, 36, 
  s_waitcnt vmcnt(6)
  KC 24, 168, 960
  LDV 234, 28, 40, 0, nt
  PPRD 1888
  s_waitcnt vmcnt(6)
  VPRE
  s_nop 1
  VCV 12
  LDV 237, 32, 36, 24, 
  s_waitcnt vmcnt(6)
  KC 36, 168, 992
  LDV 237, 33, 36, 12, 
  PPRD 1904
  s_waitcnt vmcnt(6)
  VPRE
  s_nop 1
  VCV 0
  LDV 234, 29, 40, 36, nt
  s_waitcnt vmcnt(6)
  KC 24, 168, 1024
  LDV 237, 34, 36, 0, 
  s_waitcnt vmcnt(6)
  KC 12, 168, 1056
  LDV 234, 30, 40, 24, nt
  PPRD 1920
  s_waitcnt vmcnt(6)
  VPRE
  s_nop 1
  VCV 36
  LDV 237, 35, 36, 12, 
  s_waitcnt vmcnt(6)
  KC 0, 168, 1088
  LDV 237, 36, 36, 36, 
  PPRD 1936
  s_waitcnt vmcnt(6)
  VPRE
  s_nop 1
  VCV 24
  LDV 234, 31, 40, 0, nt
  s_waitcnt vmcnt(6)
  KC 12, 168, 1120
  LDV 237, 37, 36, 24, 
  s_waitcnt vmcnt(6)
  KC 36, 168, 1152
  LDV 234, 32, 40, 12, nt
  PPRD 1952
  s_waitcnt vmcnt(6)
  VPRE
  s_nop 1
  VCV 0
  LDV 234, 33, 40, 36, nt
  s_waitcnt vmcnt(6)
  KC 24, 168, 1184
  LDV 234, 34, 40, 0, nt
  PPRD 1968
  s_waitcnt vmcnt(6)
  VPRE
  s_nop 1
  VCV 12
  LDV 234, 35, 40, 24, nt
  XPUB 672, 0, 0, 1, 86, 1, 2328, 1
  PPRD 1984
  s_waitcnt vmcnt(6)
  VPRE
  PPRD 2000
  s_nop 0
  VCV 36
  LDV 234, 36, 40, 12, nt
  s_waitcnt vmcnt(6)
  VPRE
  PPRD 2016
  s_nop 0
  VCV 0
  LDV 234, 37, 40, 36, nt
  s_waitcnt vmcnt(6)
  VPRE
  s_nop 1
  VCV 24
  DLOAD 238, 1280
  LDV 238, 21, 40, 0, 
  XLOADC 0, 86
  PPRD 2032
  s_waitcnt vmcnt(7)
  VPRE
  PPRD 2048
  s_nop 0
  VCV 12
  LDV 238, 22, 40, 24, 
  s_waitcnt vmcnt(7)
  VPRE
  s_nop 1
  VCV 36
  LDV 238, 23, 40, 12, 
  PUBP 51, 524288, 1, 2816, 2388, 1, 1, 3584
  s_waitcnt vmcnt(6)
  XUSE 0, 0, 1, 86, 11
  LDV 238, 24, 40, 36, 
  PPRD 1792
  VPRE
  PPRD 1808
  s_nop 0
  VC0V 0
  s_waitcnt vmcnt(6)
  VPRE
  PPRD 1824
  s_nop 0
  VCV 24
  LDV 238, 25, 40, 0, 
  s_waitcnt vmcnt(6)
  VPRE
  PPRD 1840
  s_nop 0
  VCV 12
  LDV 238, 26, 40, 24, 
  s_waitcnt vmcnt(6)
  VPRE
  PPRD 1856
  s_nop 0
  VCV 36
  LDV 238, 27, 40, 12, 
  s_waitcnt vmcnt(6)
  VPRE
  PPRD 1872
  s_nop 0
  VCV 0
  LDV 238, 28, 40, 36, 
  s_waitcnt vmcnt(6)
  VPRE
  PPRD 1888
  s_nop 0
  VCV 24
  LDV 238, 29, 40, 0, 
  s_waitcnt vmcnt(6)
  VPRE
  PPRD 1904
  s_nop 0
  VCV 12
  LDV 238, 30, 40, 24, 
  s_waitcnt vmcnt(6)
  VPRE
  PPRD 1920
  s_nop 0
  VCV 36
  LDV 238, 31, 40, 12, 
  s_waitcnt vmcnt(6)
  VPRE
  PPRD 1936
  s_nop 0
  VCV 0
  LDV 238, 32, 40, 36, 
  s_waitcnt vmcnt(6)
  VPRE
  PPRD 1952
  s_nop 0
  VCV 24
  LDV 238, 33, 40, 0, 
  s_waitcnt vmcnt(6)
  VPRE
  PPRD 1968
  s_nop 0
  VCV 12
  LDV 238, 34, 40, 24, 
  s_waitcnt vmcnt(6)
  VPRE
  PPRD 1984
  s_nop 0
  VCV 36
  LDV 238, 35, 40, 12, 
  s_waitcnt vmcnt(6)
  VPRE
  PPRD 2000
  s_nop 0
  VCV 0
  LDV 238, 36, 40, 36, 
  s_waitcnt vmcnt(6)
  VPRE
  PPRD 2016
  s_nop 0
  VCV 24
  LDV 238, 37, 40, 0, 
  LDV 235, 21, 36, 24, nt
  s_waitcnt vmcnt(9)
  VPRE
  PPRD 2032
  s_nop 0
  VCV 12
  LDV 235, 22, 36, 12, nt
  s_waitcnt vmcnt(9)
  VPRE
  s_nop 1
  VCV 36
  QLOAD 52, 163840, 222
  PPRD 2048
  s_waitcnt vmcnt(7)
  VPRE
  s_nop 1
  VCV 0
  PUBP 51, 0, 3, 2816, 2392, 1, 0, 2560
  s_waitcnt vmcnt(0)
  QUSE 52, 163840, 222, 168, 12
  QLOAD 53, 163840, 226
  QLOAD 52, 65536, 222
  L2V 36, 0
  s_waitcnt lgkmcnt(0)
  L2V 0, 3072
  KC2 48, 36, 168, 0
  LDV 237, 0, 36, 48, nt
  LDL 237, 1, 36, 0, nt
  s_waitcnt lgkmcnt(0)
  L2V 36, 6144
  KC2 60, 0, 168, 64
  LDV 237, 2, 36, 60, nt
  LDL 237, 3, 36, 3072, nt
  s_waitcnt lgkmcnt(0)
  L2V 0, 9216
  KC2 72, 36, 168, 128
  LDV 237, 4, 36, 72, nt
  LDL 237, 5, 36, 6144, nt
  s_waitcnt lgkmcnt(0)
  L2V 36, 12288
  KC2 84, 0, 168, 192
  LDV 237, 6, 36, 84, nt
  s_waitcnt lgkmcnt(0)
  L2V 0, 15360
  KC2 96, 36, 168, 256
  s_waitcnt lgkmcnt(0)
  L2V 36, 18432
  KC2 108, 0, 168, 320
  s_waitcnt lgkmcnt(0)
  L2V 0, 21504
  KC2 120, 36, 168, 384
  s_waitcnt lgkmcnt(0)
  L2V 36, 24576
  KC2 132, 0, 168, 448
  s_waitcnt lgkmcnt(0)
  L2V 0, 27648
  KC2 144, 36, 168, 512
  LDV 235, 23, 36, 36, nt
  LDL 237, 7, 36, 9216, nt
  s_waitcnt lgkmcnt(0)
  KC2 156, 0, 168, 576
  L2V 0, 30720
  s_waitcnt lgkmcnt(0)
  KC 0, 168, 640
  LDV 235, 24, 36, 0, nt
  s_waitcnt vmcnt(31)
  QUSE 53, 163840, 226, 180, 13
  s_waitcnt vmcnt(30)
  QUSE 52, 65536, 222, 168, 14
  XLOC 0
  PPRD 1792
  VPRE
  PPRD 1808
  s_nop 0
  VC0A 0
  VPRE
  PPRD 1824
  s_nop 0
  VCA 12
  VPRE
  PPRD 1840
  s_nop 0
  VCA 24
  VPRE
  PPRD 1856
  s_nop 0
  VCA 36
  VPRE
  PPRD 1872
  s_nop 0
  VCA 48
  VPRE
  PPRD 1888
  s_nop 0
  VCA 60
  VPRE
  PPRD 1904
  s_nop 0
  VCA 72
  VPRE
  PPRD 1920
  s_nop 0
  VCA 84
  VPRE
  PPRD 1936
  s_nop 0
  VCA 96
  VPRE
  PPRD 1952
  s_nop 0
  VCA 108
  VPRE
  PPRD 1968
  s_nop 0
  VCA 120
  VPRE
  PPRD 1984
  s_nop 0
  VCA 132
  VPRE
  PPRD 2000
  s_nop 0
  VCA 144
  VPRE
  PPRD 2016
  s_nop 0
  VCA 156
  VPRE
  PPRD 2032
  s_nop 0
  VCA 168
  VPRE
  PPRD 2048
  s_nop 0
  VCA 180
  VPRE
  PPRD 2064
  s_nop 0
  VCA 192
  VPRE
  PPRD 2080
  s_nop 0
  VCA 204
  VPRE
  PPRD 2096
  s_nop 0
  VCA 216
  VPRE
  PPRD 2112
  s_nop 0
  VCA 228
  VPRE
  s_nop 1
  VCA 240
  PUBP 50, 786432, 2, 2816, 2396, 1, 0, 3584
  KC 24, 180, 1216
  KC 12, 180, 1248
  s_waitcnt vmcnt(27)
  KC 48, 168, 0
  LDV 237, 8, 36, 96, nt
  s_waitcnt vmcnt(27)
)ASM"
R"ASM(  L2V 24, 0
  s_waitcnt lgkmcnt(0)
  KC 24, 168, 32
  LDL 237, 9, 36, 12288, nt
  s_waitcnt vmcnt(27)
  KC 60, 168, 64
  LDV 235, 25, 36, 12, nt
  s_waitcnt vmcnt(27)
  L2V 24, 3072
  s_waitcnt lgkmcnt(0)
  KC 24, 168, 96
  LDV 237, 10, 36, 108, nt
  s_waitcnt vmcnt(27)
  KC 72, 168, 128
  LDL 237, 11, 36, 15360, nt
  s_waitcnt vmcnt(27)
  L2V 24, 6144
  s_waitcnt lgkmcnt(0)
  KC 24, 168, 160
  LDV 235, 26, 36, 24, nt
  s_waitcnt vmcnt(27)
  KC 84, 168, 192
  LDV 237, 12, 36, 120, nt
  s_waitcnt vmcnt(27)
  KC 36, 180, 1280
  LDL 237, 13, 36, 18432, nt
  s_waitcnt vmcnt(27)
  L2V 36, 9216
  s_waitcnt lgkmcnt(0)
  KC 36, 168, 224
  LDV 235, 27, 36, 36, nt
  s_waitcnt vmcnt(27)
  KC 0, 180, 1312
  LDV 237, 14, 36, 132, nt
  s_waitcnt vmcnt(27)
  KC 96, 168, 256
  LDL 237, 15, 36, 21504, nt
  s_waitcnt vmcnt(27)
  L2V 0, 12288
  s_waitcnt lgkmcnt(0)
  KC 0, 168, 288
  LDV 235, 28, 36, 0, nt
  s_waitcnt vmcnt(27)
  KC 12, 180, 1344
  s_waitcnt vmcnt(24)
  KC 108, 168, 320
  s_waitcnt vmcnt(21)
  L2V 12, 15360
  s_waitcnt lgkmcnt(0)
  KC 12, 168, 352
  s_waitcnt vmcnt(18)
  KC 24, 180, 1376
  s_waitcnt vmcnt(15)
  KC 120, 168, 384
  s_waitcnt vmcnt(12)
  L2V 12, 18432
  s_waitcnt lgkmcnt(0)
  KC 12, 168, 416
  s_waitcnt vmcnt(9)
  KC 36, 180, 1408
  LDV 237, 16, 36, 144, nt
  s_waitcnt vmcnt(9)
  KC 132, 168, 448
  LDL 237, 17, 36, 24576, nt
  s_waitcnt vmcnt(9)
  L2V 24, 21504
  s_waitcnt lgkmcnt(0)
  KC 24, 168, 480
  LDV 235, 29, 36, 12, nt
  s_waitcnt vmcnt(9)
  KC 0, 180, 1440
  LDV 237, 18, 36, 156, nt
  s_waitcnt vmcnt(9)
  KC 144, 168, 512
  LDL 237, 19, 36, 27648, nt
  s_waitcnt vmcnt(9)
  L2V 36, 24576
  s_waitcnt lgkmcnt(0)
  KC 36, 168, 544
  LDV 235, 30, 36, 24, nt
  s_waitcnt vmcnt(9)
  KC 12, 180, 1472
  LDL 237, 20, 36, 30720, nt
  s_waitcnt vmcnt(9)
  KC 156, 168, 576
  LDA 238, 0, 40, 0, nt
  s_waitcnt vmcnt(9)
  L2V 0, 27648
  s_waitcnt lgkmcnt(0)
  KC 0, 168, 608
  LDA 238, 1, 40, 12, nt
  s_waitcnt vmcnt(9)
  KC 24, 180, 1504
  LDV 235, 31, 36, 36, nt
  s_waitcnt vmcnt(9)
  L2V 12, 30720
  s_waitcnt lgkmcnt(0)
  KC 12, 168, 640
  LDA 238, 2, 40, 24, nt
  s_waitcnt vmcnt(9)
  LDV 235, 32, 36, 0, nt
  XLOC 0
  PPRD 1792
  VPRE
  PPRD 1808
  s_nop 0
  VC0A 0
  s_waitcnt vmcnt(9)
  VPRE
  s_nop 1
  VCA 12
  LDA 238, 3, 40, 36, nt
  s_waitcnt vmcnt(9)
  KC 36, 180, 1536
  LDA 238, 4, 40, 48, nt
  PPRD 1824
  s_waitcnt vmcnt(9)
  VPRE
  s_nop 1
  VCA 24
  LDV 235, 33, 36, 24, nt
  s_waitcnt vmcnt(9)
  KC 0, 180, 1568
  LDA 238, 5, 40, 60, nt
  PPRD 1840
  s_waitcnt vmcnt(9)
  VPRE
  PPRD 1856
  s_nop 0
  VCA 36
  LDA 238, 6, 40, 72, nt
  s_waitcnt vmcnt(9)
  VPRE
  s_nop 1
  VCA 48
  LDV 235, 34, 36, 12, nt
  s_waitcnt vmcnt(9)
  KC 24, 180, 1600
  LDA 238, 7, 40, 84, nt
  PPRD 1872
  s_waitcnt vmcnt(9)
  VPRE
  PPRD 1888
  s_nop 0
  VCA 60
  LDA 238, 8, 40, 96, nt
  s_waitcnt vmcnt(9)
  VPRE
  s_nop 1
  VCA 72
  LDV 235, 35, 36, 36, nt
  s_waitcnt vmcnt(9)
  KC 12, 180, 1632
  LDA 238, 9, 40, 108, nt
  PPRD 1904
  s_waitcnt vmcnt(9)
  VPRE
  PPRD 1920
  s_nop 0
  VCA 84
  LDV 235, 36, 36, 0, nt
  s_waitcnt vmcnt(9)
  VPRE
  s_nop 1
  VCA 96
  LDA 238, 10, 40, 120, nt
  s_waitcnt vmcnt(9)
  KC 36, 180, 1664
  LDA 238, 11, 40, 132, nt
  PPRD 1936
  s_waitcnt vmcnt(9)
  VPRE
  s_nop 1
  VCA 108
  LDV 235, 37, 36, 24, nt
  s_waitcnt vmcnt(9)
  KC 0, 180, 1696
  LDA 238, 12, 40, 144, nt
  PPRD 1952
  s_waitcnt vmcnt(9)
  VPRE
  PPRD 1968
  s_nop 0
  VCA 120
  LDA 238, 13, 40, 156, nt
  s_waitcnt vmcnt(9)
  VPRE
  s_nop 1
  VCA 132
  LDA 238, 14, 40, 168, nt
  s_waitcnt vmcnt(9)
  KC 24, 180, 1728
  LDA 238, 15, 40, 180, nt
  PPRD 1984
  s_waitcnt vmcnt(9)
  VPRE
  PPRD 2000
  s_nop 0
  VCA 144
  LDA 238, 16, 40, 192, nt
  s_waitcnt vmcnt(9)
  VPRE
  s_nop 1
  VCA 156
  LDA 238, 17, 40, 204, nt
  XPUB 1216, 1152, 3456, 0, 85, 1, 2356, 1
  PPRD 2016
  s_waitcnt vmcnt(9)
  VPRE
  PPRD 2032
  s_nop 0
  VCA 168
  LDA 238, 18, 40, 216, nt
  s_waitcnt vmcnt(9)
  VPRE
  PPRD 2048
  s_nop 0
  VCA 180
  LDA 238, 19, 40, 228, nt
  s_waitcnt vmcnt(9)
  VPRE
  PPRD 2064
  s_nop 0
  VCA 192
  LDA 238, 20, 40, 240, nt
  s_waitcnt vmcnt(9)
  VPRE
  PPRD 2080
  s_nop 0
  VCA 204
  s_waitcnt vmcnt(6)
  VPRE
  PPRD 2096
  s_nop 0
  VCA 216
  DLOAD 233, 1536
  LDV 233, 21, 36, 12, 
  s_waitcnt vmcnt(6)
  VPRE
  s_nop 1
  VCA 228
  LDV 236, 21, 40, 36, nt
  XLOADC 3456, 85
  QLOAD 53, 98304, 222
  PPRD 2112
  s_waitcnt vmcnt(8)
  VPRE
  s_nop 1
  VCA 240
  LDV 233, 22, 36, 0, 
  PUBP 50, 0, 3, 2816, 2400, 1, 1, 2560
  s_waitcnt vmcnt(3)
  QUSE 53, 98304, 222, 168, 15
  LDV 236, 22, 40, 24, nt
  XUSE 1152, 3456, 0, 85, 16
  KC 12, 168, 672
  LDV 233, 23, 36, 12, 
  PPRD 1792
  VPRE
  s_nop 1
  VC0V 36
  s_waitcnt vmcnt(6)
  KC 0, 168, 704
  LDV 233, 24, 36, 36, 
  PPRD 1808
  s_waitcnt vmcnt(6)
  VPRE
  s_nop 1
  VCV 24
  LDV 236, 23, 40, 0, nt
  s_waitcnt vmcnt(6)
  KC 12, 168, 736
  LDV 233, 25, 36, 24, 
  s_waitcnt vmcnt(6)
  KC 36, 168, 768
  LDV 236, 24, 40, 12, nt
  PPRD 1824
  s_waitcnt vmcnt(6)
  VPRE
  s_nop 1
  VCV 0
  LDV 233, 26, 36, 36, 
  s_waitcnt vmcnt(6)
  KC 24, 168, 800
  LDV 233, 27, 36, 0, 
  PPRD 1840
  s_waitcnt vmcnt(6)
  VPRE
  s_nop 1
  VCV 12
  LDV 236, 25, 40, 24, nt
  s_waitcnt vmcnt(6)
  KC 36, 168, 832
  LDV 233, 28, 36, 12, 
  s_waitcnt vmcnt(6)
  KC 0, 168, 864
  LDV 236, 26, 40, 36, nt
  PPRD 1856
  s_waitcnt vmcnt(6)
  VPRE
  s_nop 1
  VCV 24
  LDV 233, 29, 36, 0, 
  s_waitcnt vmcnt(6)
  KC 12, 168, 896
  LDV 233, 30, 36, 24, 
  PPRD 1872
  s_waitcnt vmcnt(6)
  VPRE
  s_nop 1
  VCV 36
  LDV 236, 27, 40, 12, nt
  s_waitcnt vmcnt(6)
  KC 0, 168, 928
  LDV 233, 31, 36, 36, 
  s_waitcnt vmcnt(6)
  KC 24, 168, 960
  LDV 236, 28, 40, 0, nt
  PPRD 1888
  s_waitcnt vmcnt(6)
  VPRE
  s_nop 1
  VCV 12
  LDV 233, 32, 36, 24, 
  s_waitcnt vmcnt(6)
  KC 36, 168, 992
  LDV 233, 33, 36, 12, 
  PPRD 1904
  s_waitcnt vmcnt(6)
  VPRE
  s_nop 1
  VCV 0
  LDV 236, 29, 40, 36, nt
  s_waitcnt vmcnt(6)
  KC 24, 168, 1024
  LDV 233, 34, 36, 0, 
  s_waitcnt vmcnt(6)
  KC 12, 168, 1056
  LDV 236, 30, 40, 24, nt
  PPRD 1920
  s_waitcnt vmcnt(6)
  VPRE
  s_nop 1
  VCV 36
  LDV 233, 35, 36, 12, 
  s_waitcnt vmcnt(6)
  KC 0, 168, 1088
  LDV 233, 36, 36, 36, 
  PPRD 1936
  s_waitcnt vmcnt(6)
  VPRE
  s_nop 1
  VCV 24
  LDV 236, 31, 40, 0, nt
  s_waitcnt vmcnt(6)
  KC 12, 168, 1120
  LDV 233, 37, 36, 24, 
  s_waitcnt vmcnt(6)
  KC 36, 168, 1152
  LDV 236, 32, 40, 12, nt
  PPRD 1952
  s_waitcnt vmcnt(6)
  VPRE
  s_nop 1
  VCV 0
  LDV 236, 33, 40, 36, nt
  s_waitcnt vmcnt(6)
  KC 24, 168, 1184
  LDV 236, 34, 40, 0, nt
  PPRD 1968
  s_waitcnt vmcnt(6)
  VPRE
  s_nop 1
  VCV 12
  LDV 236, 35, 40, 24, nt
  XPUB 672, 0, 1152, 1, 87, 1, 2332, 1
  PPRD 1984
  s_waitcnt vmcnt(6)
  VPRE
  PPRD 2000
  s_nop 0
  VCV 36
  LDV 236, 36, 40, 12, nt
  s_waitcnt vmcnt(6)
  VPRE
  PPRD 2016
  s_nop 0
  VCV 0
  LDV 236, 37, 40, 36, nt
  s_waitcnt vmcnt(6)
  VPRE
  s_nop 1
  VCV 24
  DLOAD 234, 1792
  LDV 234, 21, 40, 0, 
  XLOADC 1152, 87
  PPRD 2032
  s_waitcnt vmcnt(7)
  VPRE
  PPRD 2048
)ASM"
R"ASM(  s_nop 0
  VCV 12
  LDV 234, 22, 40, 24, 
  s_waitcnt vmcnt(7)
  VPRE
  s_nop 1
  VCV 36
  LDV 234, 23, 40, 12, 
  PUBP 51, 786432, 2, 2816, 2404, 1, 1, 3584
  s_waitcnt vmcnt(6)
  XUSE 0, 1152, 1, 87, 17
  LDV 234, 24, 40, 36, 
  PPRD 1792
  VPRE
  PPRD 1808
  s_nop 0
  VC0V 0
  s_waitcnt vmcnt(6)
  VPRE
  PPRD 1824
  s_nop 0
  VCV 24
  LDV 234, 25, 40, 0, 
  s_waitcnt vmcnt(6)
  VPRE
  PPRD 1840
  s_nop 0
  VCV 12
  LDV 234, 26, 40, 24, 
  s_waitcnt vmcnt(6)
  VPRE
  PPRD 1856
  s_nop 0
  VCV 36
  LDV 234, 27, 40, 12, 
  s_waitcnt vmcnt(6)
  VPRE
  PPRD 1872
  s_nop 0
  VCV 0
  LDV 234, 28, 40, 36, 
  s_waitcnt vmcnt(6)
  VPRE
  PPRD 1888
  s_nop 0
  VCV 24
  LDV 234, 29, 40, 0, 
  s_waitcnt vmcnt(6)
  VPRE
  PPRD 1904
  s_nop 0
  VCV 12
  LDV 234, 30, 40, 24, 
  s_waitcnt vmcnt(6)
  VPRE
  PPRD 1920
  s_nop 0
  VCV 36
  LDV 234, 31, 40, 12, 
  s_waitcnt vmcnt(6)
  VPRE
  PPRD 1936
  s_nop 0
  VCV 0
  LDV 234, 32, 40, 36, 
  s_waitcnt vmcnt(6)
  VPRE
  PPRD 1952
  s_nop 0
  VCV 24
  LDV 234, 33, 40, 0, 
  s_waitcnt vmcnt(6)
  VPRE
  PPRD 1968
  s_nop 0
  VCV 12
  LDV 234, 34, 40, 24, 
  s_waitcnt vmcnt(6)
  VPRE
  PPRD 1984
  s_nop 0
  VCV 36
  LDV 234, 35, 40, 12, 
  s_waitcnt vmcnt(6)
  VPRE
  PPRD 2000
  s_nop 0
  VCV 0
  LDV 234, 36, 40, 36, 
  s_waitcnt vmcnt(6)
  VPRE
  PPRD 2016
  s_nop 0
  VCV 24
  LDV 234, 37, 40, 0, 
  LDV 237, 21, 36, 24, nt
  s_waitcnt vmcnt(9)
  VPRE
  PPRD 2032
  s_nop 0
  VCV 12
  LDV 237, 22, 36, 12, nt
  s_waitcnt vmcnt(9)
  VPRE
  s_nop 1
  VCV 36
  QLOAD 52, 196608, 222
  PPRD 2048
  s_waitcnt vmcnt(7)
  VPRE
  s_nop 1
  VCV 0
  PUBP 51, 262144, 4, 2816, 2408, 1, 0, 2560
  s_waitcnt vmcnt(0)
  QUSE 52, 196608, 222, 168, 18
  QLOAD 53, 196608, 226
  QLOAD 52, 98304, 222
  L2V 36, 0
  s_waitcnt lgkmcnt(0)
  L2V 0, 3072
  KC2 48, 36, 168, 0
  LDV 233, 0, 36, 48, nt
  LDL 233, 1, 36, 0, nt
  s_waitcnt lgkmcnt(0)
  L2V 36, 6144
  KC2 60, 0, 168, 64
  LDV 233, 2, 36, 60, nt
  LDL 233, 3, 36, 3072, nt
  s_waitcnt lgkmcnt(0)
  L2V 0, 9216
  KC2 72, 36, 168, 128
  LDV 233, 4, 36, 72, nt
  LDL 233, 5, 36, 6144, nt
  s_waitcnt lgkmcnt(0)
  L2V 36, 12288
  KC2 84, 0, 168, 192
  LDV 233, 6, 36, 84, nt
  s_waitcnt lgkmcnt(0)
  L2V 0, 15360
  KC2 96, 36, 168, 256
  s_waitcnt lgkmcnt(0)
  L2V 36, 18432
  KC2 108, 0, 168, 320
  s_waitcnt lgkmcnt(0)
  L2V 0, 21504
  KC2 120, 36, 168, 384
  s_waitcnt lgkmcnt(0)
  L2V 36, 24576
  KC2 132, 0, 168, 448
  s_waitcnt lgkmcnt(0)
  L2V 0, 27648
  KC2 144, 36, 168, 512
  LDV 237, 23, 36, 36, nt
  LDL 233, 7, 36, 9216, nt
  LDV 233, 8, 36, 96, nt
  s_waitcnt lgkmcnt(0)
  KC2 156, 0, 168, 576
  L2V 0, 30720
  s_waitcnt lgkmcnt(0)
  KC 0, 168, 640
  s_waitcnt vmcnt(31)
  QUSE 53, 196608, 226, 180, 19
  s_waitcnt vmcnt(30)
  QUSE 52, 98304, 222, 168, 20
  XLOC 0
  PPRD 1792
  VPRE
  PPRD 1808
  s_nop 0
  VC0A 0
  VPRE
  PPRD 1824
  s_nop 0
  VCA 12
  VPRE
  PPRD 1840
  s_nop 0
  VCA 24
  VPRE
  PPRD 1856
  s_nop 0
  VCA 36
  VPRE
  PPRD 1872
  s_nop 0
  VCA 48
  VPRE
  PPRD 1888
  s_nop 0
  VCA 60
  VPRE
  PPRD 1904
  s_nop 0
  VCA 72
  VPRE
  PPRD 1920
  s_nop 0
  VCA 84
  VPRE
  PPRD 1936
  s_nop 0
  VCA 96
  VPRE
  PPRD 1952
  s_nop 0
  VCA 108
  VPRE
  PPRD 1968
  s_nop 0
  VCA 120
  VPRE
  PPRD 1984
  s_nop 0
  VCA 132
  VPRE
  PPRD 2000
  s_nop 0
  VCA 144
  VPRE
  PPRD 2016
  s_nop 0
  VCA 156
  VPRE
  PPRD 2032
  s_nop 0
  VCA 168
  VPRE
  PPRD 2048
  s_nop 0
  VCA 180
  VPRE
  PPRD 2064
  s_nop 0
  VCA 192
  VPRE
  PPRD 2080
  s_nop 0
  VCA 204
  VPRE
  PPRD 2096
  s_nop 0
  VCA 216
  VPRE
  PPRD 2112
  s_nop 0
  VCA 228
  VPRE
  s_nop 1
  VCA 240
  PUBP 50, 524288, 3, 2816, 2412, 1, 0, 3584
  KC 24, 180, 1216
  KC 12, 180, 1248
  s_waitcnt vmcnt(27)
  KC 48, 168, 0
  LDL 233, 9, 36, 12288, nt
  s_waitcnt vmcnt(27)
  L2V 0, 0
  s_waitcnt lgkmcnt(0)
  KC 0, 168, 32
  LDV 233, 10, 36, 108, nt
  s_waitcnt vmcnt(27)
  KC 60, 168, 64
  LDL 233, 11, 36, 15360, nt
  s_waitcnt vmcnt(27)
  L2V 24, 3072
  s_waitcnt lgkmcnt(0)
  KC 24, 168, 96
  LDV 233, 12, 36, 120, nt
  s_waitcnt vmcnt(27)
  KC 72, 168, 128
  LDL 233, 13, 36, 18432, nt
  s_waitcnt vmcnt(27)
  L2V 12, 6144
  s_waitcnt lgkmcnt(0)
  KC 12, 168, 160
  LDV 237, 24, 36, 0, nt
  s_waitcnt vmcnt(27)
  KC 84, 168, 192
  LDV 233, 14, 36, 132, nt
  s_waitcnt vmcnt(27)
  KC 36, 180, 1280
  LDL 233, 15, 36, 21504, nt
  s_waitcnt vmcnt(27)
  L2V 24, 9216
  s_waitcnt lgkmcnt(0)
  KC 24, 168, 224
  LDV 233, 16, 36, 144, nt
  s_waitcnt vmcnt(27)
  KC 96, 168, 256
  LDL 233, 17, 36, 24576, nt
  s_waitcnt vmcnt(27)
  L2V 12, 12288
  s_waitcnt lgkmcnt(0)
  KC 12, 168, 288
  LDV 233, 18, 36, 156, nt
  s_waitcnt vmcnt(27)
  KC 108, 168, 320
  LDL 233, 19, 36, 27648, nt
  s_waitcnt vmcnt(27)
  L2V 36, 15360
  s_waitcnt lgkmcnt(0)
  KC 36, 168, 352
  s_waitcnt vmcnt(24)
  KC 120, 168, 384
  s_waitcnt vmcnt(21)
  L2V 24, 18432
  s_waitcnt lgkmcnt(0)
  KC 24, 168, 416
  s_waitcnt vmcnt(18)
  KC 0, 180, 1312
  s_waitcnt vmcnt(15)
  KC 132, 168, 448
  s_waitcnt vmcnt(12)
  L2V 12, 21504
  s_waitcnt lgkmcnt(0)
  KC 12, 168, 480
  s_waitcnt vmcnt(9)
  KC 144, 168, 512
  LDL 233, 20, 36, 30720, nt
  s_waitcnt vmcnt(9)
  L2V 36, 24576
  s_waitcnt lgkmcnt(0)
  KC 36, 168, 544
  LDA 234, 0, 40, 0, nt
  s_waitcnt vmcnt(9)
  KC 156, 168, 576
  LDA 234, 1, 40, 12, nt
  s_waitcnt vmcnt(9)
  L2V 24, 27648
  s_waitcnt lgkmcnt(0)
  KC 24, 168, 608
  LDA 234, 2, 40, 24, nt
  s_waitcnt vmcnt(9)
  L2V 0, 30720
  s_waitcnt lgkmcnt(0)
  KC 0, 168, 640
  LDA 234, 3, 40, 36, nt
  s_waitcnt vmcnt(9)
  LDA 234, 4, 40, 48, nt
  XLOC 0
  PPRD 1792
  VPRE
  PPRD 1808
  s_nop 0
  VC0A 0
  s_waitcnt vmcnt(9)
  VPRE
  PPRD 1824
  s_nop 0
  VCA 12
  LDA 234, 5, 40, 60, nt
  s_waitcnt vmcnt(9)
  VPRE
  PPRD 1840
  s_nop 0
  VCA 24
  LDA 234, 6, 40, 72, nt
  s_waitcnt vmcnt(9)
  VPRE
  PPRD 1856
  s_nop 0
  VCA 36
  LDA 234, 7, 40, 84, nt
  s_waitcnt vmcnt(9)
  VPRE
  PPRD 1872
  s_nop 0
  VCA 48
  LDA 234, 8, 40, 96, nt
  s_waitcnt vmcnt(9)
  VPRE
  PPRD 1888
  s_nop 0
  VCA 60
  LDA 234, 9, 40, 108, nt
  s_waitcnt vmcnt(9)
  VPRE
  PPRD 1904
  s_nop 0
  VCA 72
  LDA 234, 10, 40, 120, nt
  s_waitcnt vmcnt(9)
  VPRE
  PPRD 1920
  s_nop 0
  VCA 84
  LDA 234, 11, 40, 132, nt
  s_waitcnt vmcnt(9)
  VPRE
  PPRD 1936
  s_nop 0
  VCA 96
  LDA 234, 12, 40, 144, nt
  s_waitcnt vmcnt(9)
  VPRE
  PPRD 1952
  s_nop 0
  VCA 108
  LDA 234, 13, 40, 156, nt
  s_waitcnt vmcnt(9)
)ASM"
R"ASM(  VPRE
  PPRD 1968
  s_nop 0
  VCA 120
  LDA 234, 14, 40, 168, nt
  s_waitcnt vmcnt(9)
  VPRE
  PPRD 1984
  s_nop 0
  VCA 132
  LDA 234, 15, 40, 180, nt
  s_waitcnt vmcnt(9)
  VPRE
  PPRD 2000
  s_nop 0
  VCA 144
  LDA 234, 16, 40, 192, nt
  s_waitcnt vmcnt(9)
  VPRE
  PPRD 2016
  s_nop 0
  VCA 156
  LDA 234, 17, 40, 204, nt
  s_waitcnt vmcnt(9)
  VPRE
  PPRD 2032
  s_nop 0
  VCA 168
  LDA 234, 18, 40, 216, nt
  s_waitcnt vmcnt(9)
  VPRE
  PPRD 2048
  s_nop 0
  VCA 180
  LDA 234, 19, 40, 228, nt
  s_waitcnt vmcnt(9)
  VPRE
  PPRD 2064
  s_nop 0
  VCA 192
  LDA 234, 20, 40, 240, nt
  s_waitcnt vmcnt(9)
  VPRE
  PPRD 2080
  s_nop 0
  VCA 204
  s_waitcnt vmcnt(6)
  VPRE
  PPRD 2096
  s_nop 0
  VCA 216
  s_waitcnt vmcnt(3)
  VPRE
  PPRD 2112
  s_nop 0
  VCA 228
  s_waitcnt vmcnt(0)
  VPRE
  s_nop 1
  VCA 240
  LDV 237, 25, 36, 12, nt
  PUBP 50, 262144, 4, 2816, 2416, 1, 1, 2560
  s_waitcnt vmcnt(0)
  KC 12, 180, 1344
  LDV 237, 26, 36, 36, nt
  s_waitcnt vmcnt(0)
  KC 36, 180, 1376
  LDV 237, 27, 36, 24, nt
  s_waitcnt vmcnt(0)
  KC 24, 180, 1408
  LDV 237, 28, 36, 0, nt
  s_waitcnt vmcnt(0)
  KC 0, 180, 1440
  LDV 237, 29, 36, 12, nt
  s_waitcnt vmcnt(0)
  KC 12, 180, 1472
  LDV 237, 30, 36, 36, nt
  s_waitcnt vmcnt(0)
  KC 36, 180, 1504
  LDV 237, 31, 36, 24, nt
  s_waitcnt vmcnt(0)
  KC 24, 180, 1536
  LDV 237, 32, 36, 0, nt
  s_waitcnt vmcnt(0)
  KC 0, 180, 1568
  LDV 237, 33, 36, 12, nt
  s_waitcnt vmcnt(0)
  KC 12, 180, 1600
  LDV 237, 34, 36, 36, nt
  LDV 237, 35, 36, 24, nt
  LDV 237, 36, 36, 0, nt
  s_waitcnt vmcnt(6)
  KC 36, 180, 1632
  LDV 237, 37, 36, 12, nt
  s_waitcnt vmcnt(6)
  KC 24, 180, 1664
  LDV 238, 21, 40, 36, nt
  s_waitcnt vmcnt(6)
  KC 0, 180, 1696
  LDV 238, 22, 40, 24, nt
  s_waitcnt vmcnt(6)
  KC 12, 180, 1728
  LDV 238, 23, 40, 0, nt
  XPUB 1216, 1152, 2304, 1, 86, 1, 2360, 1
  XLOADC 2304, 86
  s_waitcnt vmcnt(0)
  XUSE 1152, 2304, 1, 86, 21
  LDV 238, 24, 40, 12, nt
  PPRD 1792
  VPRE
  PPRD 1808
  s_nop 0
  VC0V 36
  LDV 238, 25, 40, 36, nt
  VPRE
  PPRD 1824
  s_nop 0
  VCV 24
  LDV 238, 26, 40, 24, nt
  VPRE
  PPRD 1840
  s_nop 0
  VCV 0
  s_waitcnt vmcnt(6)
  VPRE
  PPRD 1856
  s_nop 0
  VCV 12
  LDV 238, 27, 40, 0, nt
  s_waitcnt vmcnt(6)
  VPRE
  PPRD 1872
  s_nop 0
  VCV 36
  LDV 238, 28, 40, 12, nt
  s_waitcnt vmcnt(6)
  VPRE
  PPRD 1888
  s_nop 0
  VCV 24
  LDV 238, 29, 40, 36, nt
  s_waitcnt vmcnt(6)
  VPRE
  PPRD 1904
  s_nop 0
  VCV 0
  LDV 238, 30, 40, 24, nt
  s_waitcnt vmcnt(6)
  VPRE
  PPRD 1920
  s_nop 0
  VCV 12
  LDV 238, 31, 40, 0, nt
  s_waitcnt vmcnt(6)
  VPRE
  PPRD 1936
  s_nop 0
  VCV 36
  LDV 238, 32, 40, 12, nt
  s_waitcnt vmcnt(6)
  VPRE
  PPRD 1952
  s_nop 0
  VCV 24
  LDV 238, 33, 40, 36, nt
  s_waitcnt vmcnt(6)
  VPRE
  PPRD 1968
  s_nop 0
  VCV 0
  LDV 238, 34, 40, 24, nt
  s_waitcnt vmcnt(6)
  VPRE
  PPRD 1984
  s_nop 0
  VCV 12
  LDV 238, 35, 40, 0, nt
  s_waitcnt vmcnt(6)
  VPRE
  PPRD 2000
  s_nop 0
  VCV 36
  LDV 238, 36, 40, 12, nt
  s_waitcnt vmcnt(6)
  VPRE
  PPRD 2016
  s_nop 0
  VCV 24
  LDV 238, 37, 40, 36, nt
  LDV 233, 21, 36, 24, nt
  s_waitcnt vmcnt(9)
  VPRE
  PPRD 2032
  s_nop 0
  VCV 0
  LDV 233, 22, 36, 0, nt
  s_waitcnt vmcnt(9)
  VPRE
  PPRD 2048
  s_nop 0
  VCV 12
  s_waitcnt vmcnt(6)
  VPRE
  s_nop 1
  VCV 36
  PUBP 51, 524288, 3, 2816, 2420, 1, 1, 3584
  QLOAD 52, 229376, 222
  s_waitcnt vmcnt(0)
  QUSE 52, 229376, 222, 168, 22
  QLOAD 53, 229376, 226
  L2V 12, 0
  s_waitcnt lgkmcnt(0)
  L2V 36, 3072
  KC2 48, 12, 168, 0
  LDV 233, 23, 36, 48, nt
  LDL 233, 24, 36, 0, nt
  s_waitcnt lgkmcnt(0)
  L2V 12, 6144
  KC2 60, 36, 168, 64
  LDV 233, 25, 36, 60, nt
  LDL 233, 26, 36, 3072, nt
  s_waitcnt lgkmcnt(0)
  L2V 36, 9216
  KC2 72, 12, 168, 128
  LDV 233, 27, 36, 72, nt
  LDL 233, 28, 36, 6144, nt
  s_waitcnt lgkmcnt(0)
  L2V 12, 12288
  KC2 84, 36, 168, 192
  LDV 233, 29, 36, 84, nt
  LDL 233, 30, 36, 9216, nt
  s_waitcnt lgkmcnt(0)
  L2V 36, 15360
  KC2 96, 12, 168, 256
  LDV 233, 31, 36, 96, nt
  LDL 233, 32, 36, 12288, nt
  s_waitcnt lgkmcnt(0)
  L2V 12, 18432
  KC2 108, 36, 168, 320
  LDV 233, 33, 36, 108, nt
  LDL 233, 34, 36, 15360, nt
  s_waitcnt lgkmcnt(0)
  L2V 36, 21504
  KC2 120, 12, 168, 384
  s_waitcnt lgkmcnt(0)
  L2V 12, 24576
  KC2 132, 36, 168, 448
  s_waitcnt lgkmcnt(0)
  L2V 36, 27648
  KC2 144, 12, 168, 512
  s_waitcnt lgkmcnt(0)
  KC2 156, 36, 168, 576
  L2V 12, 30720
  s_waitcnt lgkmcnt(0)
  KC 12, 168, 640
  s_waitcnt vmcnt(36)
  QUSE 53, 229376, 226, 180, 23
  KC 24, 180, 1216
  KC 0, 180, 1248
  s_waitcnt vmcnt(33)
  KC 48, 180, 1280
  LDV 233, 35, 36, 120, nt
  XLOC 0
  PPRD 1792
  VPRE
  PPRD 1808
  s_nop 0
  VC0A 0
  VPRE
  PPRD 1824
  s_nop 0
  VCA 12
  VPRE
  PPRD 1840
  s_nop 0
  VCA 24
  VPRE
  PPRD 1856
  s_nop 0
  VCA 36
  VPRE
  PPRD 1872
  s_nop 0
  VCA 48
  VPRE
  PPRD 1888
  s_nop 0
  VCA 60
  VPRE
  PPRD 1904
  s_nop 0
  VCA 72
  VPRE
  PPRD 1920
  s_nop 0
  VCA 84
  VPRE
  PPRD 1936
  s_nop 0
  VCA 96
  VPRE
  PPRD 1952
  s_nop 0
  VCA 108
  VPRE
  PPRD 1968
  s_nop 0
  VCA 120
  VPRE
  PPRD 1984
  s_nop 0
  VCA 132
  VPRE
  PPRD 2000
  s_nop 0
  VCA 144
  VPRE
  PPRD 2016
  s_nop 0
  VCA 156
  VPRE
  PPRD 2032
  s_nop 0
  VCA 168
  VPRE
  PPRD 2048
  s_nop 0
  VCA 180
  VPRE
  PPRD 2064
  s_nop 0
  VCA 192
  VPRE
  PPRD 2080
  s_nop 0
  VCA 204
  VPRE
  PPRD 2096
  s_nop 0
  VCA 216
  VPRE
  PPRD 2112
  s_nop 0
  VCA 228
  VPRE
  s_nop 1
  VCA 240
  PUBP 50, 786432, 4, 2816, 2424, 1, 0, 3584
  s_waitcnt vmcnt(33)
  L2V 36, 0
  s_waitcnt lgkmcnt(0)
  KC 36, 180, 1312
  LDL 233, 36, 36, 18432, nt
  s_waitcnt vmcnt(33)
  KC 60, 180, 1344
  LDV 233, 37, 36, 132, nt
  s_waitcnt vmcnt(33)
  L2V 12, 3072
  s_waitcnt lgkmcnt(0)
  KC 12, 180, 1376
  LDL 234, 21, 40, 21504, nt
  s_waitcnt vmcnt(33)
  KC 72, 180, 1408
  LDV 234, 22, 40, 144, nt
  s_waitcnt vmcnt(33)
  L2V 24, 6144
  s_waitcnt lgkmcnt(0)
  KC 24, 180, 1440
  LDL 234, 23, 40, 24576, nt
  s_waitcnt vmcnt(33)
  KC 84, 180, 1472
  LDV 234, 24, 40, 156, nt
  s_waitcnt vmcnt(33)
  L2V 0, 9216
  s_waitcnt lgkmcnt(0)
  KC 0, 180, 1504
  LDL 234, 25, 40, 27648, nt
  s_waitcnt vmcnt(33)
  KC 96, 180, 1536
  LDL 234, 26, 40, 30720, nt
  s_waitcnt vmcnt(33)
  L2V 36, 12288
  s_waitcnt lgkmcnt(0)
  KC 36, 180, 1568
  LDA 234, 27, 40, 72, nt
  s_waitcnt vmcnt(33)
  KC 108, 180, 1600
  LDA 234, 28, 40, 84, nt
  s_waitcnt vmcnt(33)
  L2V 12, 15360
  s_waitcnt lgkmcnt(0)
  KC 12, 180, 1632
  LDA 234, 29, 40, 96, nt
  s_waitcnt vmcnt(33)
  KC 120, 180, 1664
  LDA 234, 30, 40, 108, nt
  s_waitcnt vmcnt(33)
  L2V 24, 18432
  s_waitcnt lgkmcnt(0)
  KC 24, 180, 1696
)ASM"
R"ASM(  LDA 234, 31, 40, 120, nt
  s_waitcnt vmcnt(33)
  KC 132, 180, 1728
  LDA 234, 32, 40, 132, nt
  XPUB 1216, 1152, 3456, 1, 87, 1, 2364, 1
  LDA 234, 33, 40, 144, nt
  LDA 234, 34, 40, 156, nt
  XLOADC 3456, 87
  LDA 234, 35, 40, 168, nt
  LDA 234, 36, 40, 180, nt
  LDA 234, 37, 40, 192, nt
  s_waitcnt vmcnt(9)
  XUSE 1152, 3456, 1, 87, 24
  PPRD 1792
  L2V 0, 21504
  VPRE
  PPRD 1808
  s_nop 0
  VC0V 0
  VPRE
  PPRD 1824
  s_nop 0
  VCV 144
  L2V 36, 24576
  VPRE
  PPRD 1840
  s_nop 0
  VCV 36
  VPRE
  PPRD 1856
  s_nop 0
  VCV 156
  L2V 12, 27648
  VPRE
  PPRD 1872
  s_nop 0
  VCV 12
  L2V 24, 30720
  VPRE
  PPRD 1888
  s_nop 0
  VCV 24
  VPRE
  PPRD 1904
  s_nop 0
  VCA 72
  VPRE
  PPRD 1920
  s_nop 0
  VCA 84
  VPRE
  PPRD 1936
  s_nop 0
  VCA 96
  VPRE
  PPRD 1952
  s_nop 0
  VCA 108
  VPRE
  PPRD 1968
  s_nop 0
  VCA 120
  VPRE
  PPRD 1984
  s_nop 0
  VCA 132
  VPRE
  PPRD 2000
  s_nop 0
  VCA 144
  VPRE
  PPRD 2016
  s_nop 0
  VCA 156
  s_waitcnt vmcnt(6)
  VPRE
  PPRD 2032
  s_nop 0
  VCA 168
  s_waitcnt vmcnt(3)
  VPRE
  PPRD 2048
  s_nop 0
  VCA 180
  s_waitcnt vmcnt(0)
  VPRE
  s_nop 1
  VCA 192
  PUBP 51, 786432, 4, 2816, 2428, 1, 1, 3584
  s_waitcnt vmcnt(0)
  s_endpgm
.Lstream_end:
.fill 256, 4, 0xBF800000
.subsection 0

)ASM"
    :
    : "s"((unsigned)(unsigned long long)a.k), "s"((unsigned)((unsigned long long)a.k >> 32)), "s"((unsigned)(unsigned long long)a.v), "s"((unsigned)((unsigned long long)a.v >> 32)),
      "s"((unsigned)(unsigned long long)a.ws), "s"((unsigned)((unsigned long long)a.ws >> 32)), "s"(W_END),
      "s"(xin), "s"(xout), "s"(pr), "s"(pn), "s"(qr), "s"(qn), "s"(ldsb + L_KSLOT + w * L_KW), "s"(ldsb + L_DESC), "s"(nN), "s"(ldsb + L_MISC + w * WAVE_MISC),
      "s"(W_DBG + (unsigned)(sc * 4 + w) * 256u), "s"(ldsb + L_CU), "s"(ldsb + L_MISC), "s"(ldsb + L_CU + w * 288), "s"(__builtin_amdgcn_readfirstlane(k7 != 3 ? 1 : 0)), "s"(nR)
    : "v255", "a255", "s36", "s37", "s38", "s39", "s40", "s41", "s42", "s43", "s44", "s45", "s46", "s47", "s48", "s49", "s50", "s51", "s52", "s53", "s54", "s55", "s56", "s57", "s58", "s59", "s60", "s61", "s62", "s63", "s64", "s65",
      "s66", "s67", "s68", "s69", "s70", "s71", "s72", "s73", "s74", "s75", "s76", "s77", "s78", "s79", "s80", "s81", "s82", "s83", "s84", "s85", "s86", "s87", "s88", "s89", "vcc", "scc", "memory");
  __builtin_unreachable();
}

__global__ void __launch_bounds__(512) prep(const char* k, const char* v, char* ws) {
  const int lane = threadIdx.x & 63, wv = __builtin_amdgcn_readfirstlane(threadIdx.x >> 6);
  if (wv >= 4) {
    for (unsigned o = (blockIdx.x * 256 + threadIdx.x - 256) * 16; o < W_ZERO_END; o += 256 * 256 * 16) *(v4u*)(ws + o) = v4u{0u, 0u, 0u, 0u};
    return;
  }
  const int slot = blockIdx.x + 256 * (wv >> 1), voter = wv & 1;
  if (slot < 2 * NPG) {
    const unsigned long long base = (unsigned long long)(slot < NPG ? k : v);
    const int pg = slot < NPG ? slot : slot - NPG;
    const unsigned long long ps_ = ((base >> 21) + pg) << 21;
    unsigned long long lo = ps_ > base ? ps_ : base, hi = ps_ + (2u << 20);
    if (hi > base + KV_BYTES) hi = base + KV_BYTES;
    lo = (lo + 16383) & ~16383ull;
    int vote = 0;
    if (lo + 131072 <= hi) {
      const unsigned long long pb = lo + voter * 65536;
      unsigned tm[4], sink;
      asm volatile("global_load_dword %0, %1, off sc1 nt\n\ts_waitcnt vmcnt(0)" : "=v"(sink) : "v"(pb + 49152 + 2048) : "memory");
      __builtin_amdgcn_s_sleep(4);
#pragma unroll
      for (int i = 0; i < 4; ++i) {
        unsigned long long p = pb + (i & 1) * 8192 + (i >> 1) * (16384 + 4096 + 512);
        for (int tr = 0; tr < 8; ++tr, p += 128) {
          unsigned long long t0, t1;
          asm volatile("s_memtime %0\n\ts_waitcnt lgkmcnt(0)\n\tglobal_load_dword %2, %3, off nt\n\ts_waitcnt vmcnt(0)\n\ts_memtime %1\n\ts_waitcnt lgkmcnt(0)"
                       : "=&s"(t0), "=&s"(t1), "=&v"(sink) : "v"(p) : "memory");
          tm[i] = (unsigned)(t1 - t0);
          if (tm[i] >= 360u) break;
        }
      }
      const int d = (int)min(tm[1], tm[3]) - (int)min(tm[0], tm[2]);
      vote = (xcc_id() >> 2) ? -d : d;
    }
    if (lane == 0) __hip_atomic_store((int*)(ws + W_PROBE) + slot * 2 + voter, vote, __ATOMIC_RELAXED, __HIP_MEMORY_SCOPE_AGENT);
  }
}

extern "C" void kernel_launch(void* const* d_in, const int* in_sizes, int n_in, void* d_out, int out_size, void* d_ws, size_t ws_size, hipStream_t stream) {
  Args a;
  a.x = (const unsigned short*)d_in[0]; a.k = (const char*)d_in[1]; a.v = (const char*)d_in[2];
  a.wq = (const unsigned*)d_in[3]; a.wo = (const unsigned*)d_in[4];
  a.wg = (const unsigned*)d_in[6]; a.wu = (const unsigned*)d_in[7]; a.wd = (const unsigned*)d_in[8];
  a.out = (unsigned short*)d_out; a.ws = (char*)d_ws;
  (void)hipFuncSetAttribute((const void*)fused_decode, hipFuncAttributeMaxDynamicSharedMemorySize, L_TOTAL);
  prep<<<256, 512, 0, stream>>>(a.k, a.v, a.ws);
  fused_decode<<<256, 256, L_TOTAL, stream>>>(a);
}
```
